```python
import torch

def _softmax(x, axis=-1):
    return torch.softmax(x, dim=axis)

def _gelu(x):
    return torch.nn.functional.gelu(x, approximate='tanh')

def _silu(x):
    return torch.nn.functional.silu(x)

def _sigmoid(x):
    return torch.sigmoid(x)

def _layernorm(x, axis=-1, eps=1e-05):
    m = x.mean(dim=axis, keepdim=True)
    v = x.var(dim=axis, keepdim=True, unbiased=False)
    return (x - m) / torch.sqrt(v + eps)

def _scatter_add(tgt, idx, src, axis=0):
    return tgt.clone().index_add_(axis, idx, src)

def _segment_sum(src, seg, n):
    out = torch.zeros((n,) + src.shape[1:], dtype=src.dtype)
    return out.index_add_(0, seg, src)

def _pad(x, pw):
    flat = tuple((v for pair in reversed(pw) for v in pair))
    return torch.nn.functional.pad(x, flat)
kernel_name = 'standalone__autoregressive_conv_wavenet__speech'

def setup_inputs():

    _w = lambda a: (lambda t: t.to(torch.bfloat16) if t.is_floating_point() else t)(torch.as_tensor(a))
    B, T, D, K = (2, 16000, 144, 5)
    return {'x': _w((lambda *s: torch.randn(*s, dtype=torch.bfloat16) * 0.1)(B, T, D)), 'w': _w((lambda *s: torch.randn(*s, dtype=torch.bfloat16) * 0.1)(K, D, 2 * D)), 'w_o': _w((lambda *s: torch.randn(*s, dtype=torch.bfloat16) * 0.05)(D, D))}

def reference(x, w, w_o):
    B, T, D = x.shape
    K = w.shape[0]
    dil = 2
    pad = (K - 1) * dil
    xp = _pad(x, ((0, 0), (pad, 0), (0, 0)))
    h = torch.einsum('btc,cd->btd', xp[:, pad:pad + T, :], w[0])
    for i in range(1, K):
        off = pad - i * dil
        h = h + torch.einsum('btc,cd->btd', xp[:, off:off + T, :], w[i])
    f = h[..., :D].clone()
    g = h[..., D:].clone()
    z = torch.tanh(f) * _sigmoid(g)
    return z @ w_o
```

```cpp
#include <hip/hip_runtime.h>
#include <hip/hip_bf16.h>

static __device__ __forceinline__ float bf2f(unsigned short u) { return __uint_as_float(((unsigned)u) << 16); }

__global__ __launch_bounds__(256) void naive_kernel(const unsigned short* __restrict__ x, const unsigned short* __restrict__ w,
                             const unsigned short* __restrict__ wo, __hip_bfloat16* __restrict__ out) {
    __shared__ float xs[24][144];
    __shared__ float zs[16][144];
    const int t0 = blockIdx.x * 16;
    const int tb = t0 % 16000;
    for (int i = threadIdx.x; i < 24 * 144; i += 256) {
        int r = i / 144, c = i % 144;
        int tt = tb - 8 + r;
        xs[r][c] = tt >= 0 ? bf2f(x[(size_t)(t0 - 8 + r) * 144 + c]) : 0.f;
    }
    __syncthreads();
    for (int i = threadIdx.x; i < 16 * 144; i += 256) {
        int r = i / 144, c = i % 144;
        float f = 0.f, g = 0.f;
        for (int k = 0; k < 5; ++k)
            for (int cc = 0; cc < 144; ++cc) {
                float xv = xs[r + 8 - 2 * k][cc];
                f += xv * bf2f(w[(k * 144 + cc) * 288 + c]);
                g += xv * bf2f(w[(k * 144 + cc) * 288 + 144 + c]);
            }
        float z = tanhf(f) * (1.f / (1.f + expf(-g)));
        zs[r][c] = __bfloat162float(__float2bfloat16(z));
    }
    __syncthreads();
    for (int i = threadIdx.x; i < 16 * 144; i += 256) {
        int r = i / 144, c = i % 144;
        float o = 0.f;
        for (int cc = 0; cc < 144; ++cc) o += zs[r][cc] * bf2f(wo[cc * 144 + c]);
        out[(size_t)(t0 + r) * 144 + c] = __float2bfloat16(o);
    }
}

extern "C" void kernel_launch(void* const* d_in, const int* in_sizes, int n_in,
                              void* d_out, int out_size, void* d_ws, size_t ws_size,
                              hipStream_t stream) {
    naive_kernel<<<2000, 256, 0, stream>>>((const unsigned short*)d_in[0], (const unsigned short*)d_in[1],
                                          (const unsigned short*)d_in[2], (__hip_bfloat16*)d_out);
}
```

```cpp
#include <hip/hip_runtime.h>
#include <hip/hip_bf16.h>
static __device__ __forceinline__ float bf2f(unsigned short u) { return __uint_as_float(((unsigned)u) << 16); }
__global__ __launch_bounds__(1024, 1) void naive_kernel(const unsigned short* __restrict__ x, const unsigned short* __restrict__ w,
                             const unsigned short* __restrict__ wo, __hip_bfloat16* __restrict__ out) {
    unsigned long long tstart = __builtin_amdgcn_s_memrealtime();
    __shared__ float smem[5760];
    float (*xs)[144] = (float (*)[144])smem;
    float (*zs)[144] = (float (*)[144])(smem + 24 * 144);

    for (int sub = 0; sub < 8; ++sub) {
    const int t0 = blockIdx.x * (8 * 16) + sub * 16;
    const int tb = t0 % 16000;
    __syncthreads();
    for (int i = threadIdx.x; i < 24 * 144; i += 1024) {
        int r = i / 144, c = i % 144;
        int tt = tb - 8 + r;
        xs[r][c] = tt >= 0 ? bf2f(x[(size_t)(t0 - 8 + r) * 144 + c]) : 0.f;
    }
    __syncthreads();
    for (int i = threadIdx.x; i < 16 * 144; i += 1024) {
        int r = i / 144, c = i % 144;
        float f = 0.f, g = 0.f;
        for (int k = 0; k < 5; ++k)
            for (int cc = 0; cc < 144; ++cc) {
                float xv = xs[r + 8 - 2 * k][cc];
                f += xv * bf2f(w[(k * 144 + cc) * 288 + c]);
                g += xv * bf2f(w[(k * 144 + cc) * 288 + 144 + c]);
            }
        float z = tanhf(f) * (1.f / (1.f + expf(-g)));
        zs[r][c] = __bfloat162float(__float2bfloat16(z));
    }
    __syncthreads();
    for (int i = threadIdx.x; i < 16 * 144; i += 1024) {
        int r = i / 144, c = i % 144;
        float o = 0.f;
        for (int cc = 0; cc < 144; ++cc) o += zs[r][cc] * bf2f(wo[cc * 144 + c]);
        out[(size_t)(t0 + r) * 144 + c] = __float2bfloat16(o);
    }
    }
    if (threadIdx.x == 999) smem[5760 - 1] = 1.f;
    __builtin_amdgcn_s_waitcnt(0);
    while (__builtin_amdgcn_s_memrealtime() - tstart < 300000ull) __builtin_amdgcn_s_sleep(2);
}
extern "C" void kernel_launch(void* const* d_in, const int* in_sizes, int n_in, void* d_out, int out_size, void* d_ws, size_t ws_size, hipStream_t stream) {
    naive_kernel<<<250, 1024, 0, stream>>>((const unsigned short*)d_in[0], (const unsigned short*)d_in[1], (const unsigned short*)d_in[2], (__hip_bfloat16*)d_out);
}
```

```cpp
#include <hip/hip_runtime.h>

extern "C" __global__ __launch_bounds__(256, 1) void wavenet_fused_kernel(const void* x, const void* w, const void* wo, void* out) {
  __shared__ __attribute__((aligned(16))) char smem[163840];
  unsigned tid = threadIdx.x; unsigned bid = blockIdx.x;
  asm volatile(R"ASM(
  s_load_dwordx8 s[4:11], s[0:1], 0x0
  s_lshr_b32 s55, s14, 3
  s_and_b32 s36, s14, 7
  s_lshr_b32 s37, s14, 3
  s_mul_i32 s14, s36, 31
  s_add_u32 s14, s14, s37
  s_min_u32 s36, s36, 2
  s_add_u32 s14, s14, s36
  s_getpc_b64 s[52:53]
  CODE_START_wavenet_fused_kernel:
  v_lshlrev_b32 v16, 7, v0
  v_min_u32 v16, CODE_END_wavenet_fused_kernel-CODE_START_wavenet_fused_kernel-4, v16
  global_load_dword v255, v16, s[52:53]
  v_and_b32 v1, 63, v0
  v_lshrrev_b32 v15, 6, v0
  s_nop 0
  v_readfirstlane_b32 s15, v15
  v_lshrrev_b32 v14, 1, v1
  v_mul_u32_u24 v14, 288, v14
  v_and_b32 v13, 1, v1
  v_lshl_add_u32 v14, v13, 4, v14
  s_cmp_ge_u32 s14, 125
  s_cselect_b32 s36, 1, 0
  s_mul_i32 s51, s36, 4608000
  s_mov_b32 s22, 4608000
  s_mov_b32 s23, 0x20000
  s_mul_i32 s38, s36, 125
  s_sub_u32 s38, s14, s38
  s_mul_i32 s38, s38, 36864
  s_sub_u32 s38, s38, 2304
  s_mul_i32 s37, s15, 9216
  s_add_u32 s37, s37, s38
  v_add_u32 v15, s37, v14
  s_lshl_b32 s39, s15, 10
  s_add_u32 s39, s39, 110976
  s_add_u32 m0, s39, 40960
  s_mov_b32 s57, 256
  s_waitcnt lgkmcnt(0)
  s_add_u32 s20, s4, s51
  s_addc_u32 s21, s5, 0
  s_and_b32 s21, s21, 0xffff
  buffer_load_dwordx4 v15, s[20:23], s57 offen lds
  s_add_u32 s38, s38, 36864
  v_add_u32 v14, s38, v14
  s_mov_b32 m0, 156032
  s_mov_b64 exec, 0xffff
  buffer_load_dwordx4 v14, s[20:23], s57 offen lds
  s_mov_b64 exec, -1
  v_lshlrev_b32 v2, 4, v1
  s_mov_b32 s16, s6
  s_and_b32 s17, s7, 0xffff
  s_mov_b32 s18, 414720
  s_mov_b32 s19, 0x20000
  s_mul_i32 s40, s15, 4608
  s_lshr_b32 s35, s15, 1
  s_and_b32 s34, s15, 1
  s_lshl_b32 s41, s35, 5
  s_add_u32 s41, s41, s40
  s_mul_i32 s56, s35, 73728
  s_add_u32 s56, s56, s40
  s_add_u32 m0, s41, 0
  s_add_u32 s45, s40, 405504
  buffer_load_dwordx4 v2, s[16:19], s45 offen lds
  buffer_load_dwordx4 v2, s[16:19], s45 offen offset:1024 lds
  buffer_load_dwordx4 v2, s[16:19], s45 offen offset:2048 lds
  buffer_load_dwordx4 v2, s[16:19], s45 offen offset:3072 lds
  buffer_load_dwordx4 v2, s[16:19], s45 offen offset:3584 lds
  s_add_u32 m0, s41, 18496
  s_add_u32 s45, s56, 73728
  buffer_load_dwordx4 v2, s[16:19], s45 offen lds
  buffer_load_dwordx4 v2, s[16:19], s45 offen offset:1024 lds
  buffer_load_dwordx4 v2, s[16:19], s45 offen offset:2048 lds
  buffer_load_dwordx4 v2, s[16:19], s45 offen offset:3072 lds
  buffer_load_dwordx4 v2, s[16:19], s45 offen offset:3584 lds
  s_add_u32 m0, s41, 36992
  s_add_u32 s45, s56, 239616
  buffer_load_dwordx4 v2, s[16:19], s45 offen lds
  buffer_load_dwordx4 v2, s[16:19], s45 offen offset:1024 lds
  buffer_load_dwordx4 v2, s[16:19], s45 offen offset:2048 lds
  buffer_load_dwordx4 v2, s[16:19], s45 offen offset:3072 lds
  buffer_load_dwordx4 v2, s[16:19], s45 offen offset:3584 lds
  s_add_u32 m0, s41, 55488
  s_add_u32 s45, s40, 0
  buffer_load_dwordx4 v2, s[16:19], s45 offen lds
  buffer_load_dwordx4 v2, s[16:19], s45 offen offset:1024 lds
  buffer_load_dwordx4 v2, s[16:19], s45 offen offset:2048 lds
  buffer_load_dwordx4 v2, s[16:19], s45 offen offset:3072 lds
  buffer_load_dwordx4 v2, s[16:19], s45 offen offset:3584 lds
  s_sub_u32 s38, s38, 36864
  s_lshl_b32 s37, s15, 10
  s_add_u32 s37, s37, s38
  v_add_u32 v160, s37, v2
  v_add_u32 v161, 4096, v160
  s_add_u32 m0, s39, 0
  s_mov_b32 s57, 0
  buffer_load_dwordx4 v160, s[20:23], s57 offen lds
  s_add_u32 m0, s39, 4096
  s_mov_b32 s57, 0
  buffer_load_dwordx4 v161, s[20:23], s57 offen lds
  s_add_u32 m0, s39, 8192
  s_mov_b32 s57, 4096
  buffer_load_dwordx4 v161, s[20:23], s57 offen lds
  v_lshrrev_b32 v232, 4, v1
  v_bfe_u32 v233, v1, 2, 2
  v_and_b32 v234, 3, v1
  v_and_b32 v235, 15, v1
  v_and_b32 v236, 1, v232
  v_lshrrev_b32 v237, 1, v232
  v_lshlrev_b32 v238, 3, v234
  s_xor_b32 s38, s35, 1
  s_lshl_b32 s36, s34, 6
  s_lshl_b32 s37, s35, 5
  s_add_u32 s37, s37, s36
  s_lshl_b32 s47, s38, 5
  s_add_u32 s47, s47, s36
  s_mul_i32 s46, s37, 288
  s_lshl_b32 s37, s37, 5
  s_lshl_b32 s47, s47, 5
  v_lshlrev_b32 v240, 5, v235
  v_lshl_add_u32 v241, v232, 3, v240
  v_add_u32 v241, 151936, v241
  v_add_u32 v11, s37, v241
  v_add_u32 v12, s47, v241
  v_lshl_add_u32 v241, v237, 4, v240
  v_lshlrev_b32 v243, 6, v236
  v_sub_u32 v243, v241, v243
  v_add_u32 v243, 151936, v243
  v_add_u32 v8, s37, v243
  v_add_u32 v10, s47, v243
  v_mul_u32_u24 v242, 288, v235
  v_lshl_add_u32 v242, v236, 5, v242
  v_lshl_add_u32 v242, v237, 4, v242
  v_add_u32 v242, 110976, v242
  s_mul_i32 s37, s37, 9
  s_mul_i32 s47, s47, 9
  v_add_u32 v7, s37, v242
  v_add_u32 v9, s47, v242
  s_mul_i32 s36, s35, 160
  v_lshl_add_u32 v239, v232, 2, v233
  v_mul_u32_u24 v240, 576, v239
  v_add_u32 v244, v240, v238
  v_add_u32 v245, s36, v244
  v_lshl_add_u32 v246, v237, 3, v233
  v_mul_u32_u24 v246, 576, v246
  v_mul_u32_u24 v247, 9248, v236
  v_add3_u32 v5, v246, v247, v238
  v_add_u32 v6, 73984, v5
  v_add_u32 v3, s36, v5
  v_add_u32 v4, s36, v6
  s_waitcnt vmcnt(18)
  s_barrier
  ds_read_b64 a[32:33], v11
  ds_read_b64_tr_b16 a[0:1], v245
  ds_read_b64 a[36:37], v11 offset:512
  ds_read_b64 a[40:41], v12
  ds_read_b64 a[44:45], v12 offset:512
  ds_read_b64_tr_b16 a[4:5], v245 offset:288
  ds_read_b64_tr_b16 a[8:9], v245 offset:32
  s_waitcnt lgkmcnt(5)
  v_mfma_f32_16x16x16_bf16 v[16:19], a[0:1], a[32:33], 0
  s_add_u32 m0, s39, 12288
  s_mov_b32 s57, 8192
  s_waitcnt lgkmcnt(4)
  v_mfma_f32_16x16x16_bf16 v[20:23], a[0:1], a[36:37], 0
  buffer_load_dwordx4 v161, s[20:23], s57 offen lds
  s_waitcnt lgkmcnt(3)
  v_mfma_f32_16x16x16_bf16 v[24:27], a[0:1], a[40:41], 0
  ds_read_b64_tr_b16 a[12:13], v245 offset:320
  s_waitcnt lgkmcnt(3)
  v_mfma_f32_16x16x16_bf16 v[28:31], a[0:1], a[44:45], 0
  ds_read_b64_tr_b16 a[16:17], v245 offset:64
  s_waitcnt lgkmcnt(3)
  v_mfma_f32_16x16x16_bf16 v[44:47], a[4:5], a[44:45], 0
  s_add_u32 m0, s39, 16384
  s_mov_b32 s57, 12288
  s_mov_b32 s24, s8
  v_mfma_f32_16x16x16_bf16 v[40:43], a[4:5], a[40:41], 0
  buffer_load_dwordx4 v161, s[20:23], s57 offen lds
  s_and_b32 s25, s9, 0xffff
  v_mfma_f32_16x16x16_bf16 v[36:39], a[4:5], a[36:37], 0
  ds_read_b64_tr_b16 a[20:21], v245 offset:352
  s_mov_b32 s26, 41472
  v_mfma_f32_16x16x16_bf16 v[32:35], a[4:5], a[32:33], 0
  ds_read_b64_tr_b16 a[24:25], v245 offset:96
  s_waitcnt lgkmcnt(4)
  v_mfma_f32_16x16x16_bf16 v[48:51], a[8:9], a[32:33], 0
  ds_read_b64_tr_b16 a[0:1], v245 offset:384
  s_mov_b32 s27, 0x20000
  v_mfma_f32_16x16x16_bf16 v[52:55], a[8:9], a[36:37], 0
  s_add_u32 m0, s39, 20480
  s_mov_b32 s57, 16384
  s_mov_b32 s28, s10
  v_mfma_f32_16x16x16_bf16 v[56:59], a[8:9], a[40:41], 0
  buffer_load_dwordx4 v161, s[20:23], s57 offen lds
  s_and_b32 s29, s11, 0xffff
  v_mfma_f32_16x16x16_bf16 v[60:63], a[8:9], a[44:45], 0
  ds_read_b64_tr_b16 a[4:5], v244 offset:128
  s_waitcnt lgkmcnt(4)
  v_mfma_f32_16x16x16_bf16 v[76:79], a[12:13], a[44:45], 0
  s_mov_b32 s30, 9216000
  s_mov_b32 s31, 0x20000
  s_mov_b32 s42, 0xc038aa3b
  v_mfma_f32_16x16x16_bf16 v[72:75], a[12:13], a[40:41], 0
  ds_read_b64_tr_b16 a[8:9], v244 offset:416
  s_mov_b32 s43, 0xbfb8aa3b
  v_mfma_f32_16x16x16_bf16 v[68:71], a[12:13], a[36:37], 0
  s_add_u32 m0, s39, 24576
  s_mov_b32 s57, 20480
  s_mov_b32 s44, 0x80000000
  v_mfma_f32_16x16x16_bf16 v[64:67], a[12:13], a[32:33], 0
  buffer_load_dwordx4 v161, s[20:23], s57 offen lds
  v_mul_u32_u24 v240, 288, v239
  v_mfma_f32_16x16x16_bf16 v[80:83], a[16:17], a[32:33], 0
  v_add_u32 v240, v240, v238
  v_add_u32 v250, 129408, v240
  v_mfma_f32_16x16x16_bf16 v[84:87], a[16:17], a[36:37], 0
  s_mul_i32 s37, s35, 23040
  s_add_u32 s37, s37, 110976
  v_add_u32 v248, s37, v240
  v_mfma_f32_16x16x16_bf16 v[88:91], a[16:17], a[40:41], 0
  s_mul_i32 s37, s38, 23040
  s_add_u32 s37, s37, 110976
  v_add_u32 v249, s37, v240
  v_mfma_f32_16x16x16_bf16 v[92:95], a[16:17], a[44:45], 0
  s_add_u32 m0, s39, 28672
  s_mov_b32 s57, 24576
  s_waitcnt lgkmcnt(0)
  v_mfma_f32_16x16x16_bf16 v[108:111], a[20:21], a[44:45], 0
  buffer_load_dwordx4 v161, s[20:23], s57 offen lds
  s_lshl_b32 s36, s15, 12
  v_mfma_f32_16x16x16_bf16 v[104:107], a[20:21], a[40:41], 0
  s_add_u32 s36, s36, 92480
  v_add_u32 v251, s36, v2
  s_xor_b32 s36, s15, 2
  v_mfma_f32_16x16x16_bf16 v[100:103], a[20:21], a[36:37], 0
  s_lshl_b32 s36, s36, 12
  s_add_u32 s36, s36, 92480
  v_add_u32 v252, s36, v2
  v_mfma_f32_16x16x16_bf16 v[96:99], a[20:21], a[32:33], 0
  s_waitcnt vmcnt(18)
  s_barrier
  s_mul_i32 s36, s14, 36864
  v_mfma_f32_16x16x16_bf16 v[112:115], a[24:25], a[32:33], 0
  s_add_u32 m0, s39, 32768
  s_mov_b32 s57, 28672
  s_add_u32 s36, s36, s46
  v_mfma_f32_16x16x16_bf16 v[116:119], a[24:25], a[36:37], 0
  buffer_load_dwordx4 v161, s[20:23], s57 offen lds
  ds_read_b128 a[48:51], v8 offset:256
  v_mfma_f32_16x16x16_bf16 v[120:123], a[24:25], a[40:41], 0
  ds_read_b128 a[52:55], v8 offset:768
  v_mul_u32_u24 v240, 288, v235
  v_mfma_f32_16x16x16_bf16 v[124:127], a[24:25], a[44:45], 0
  ds_read_b128 a[56:59], v10 offset:256
  v_add_u32 v240, s36, v240
  v_mfma_f32_16x16x16_bf16 v[140:143], a[0:1], a[44:45], 0
  ds_read_b128 a[60:63], v10 offset:768
  v_lshlrev_b32 v241, 5, v236
  v_mfma_f32_16x16x16_bf16 v[136:139], a[0:1], a[40:41], 0
  s_add_u32 m0, s39, 36864
  s_mov_b32 s57, 32768
  v_lshl_add_u32 v241, v237, 4, v241
  v_mfma_f32_16x16x16_bf16 v[132:135], a[0:1], a[36:37], 0
  buffer_load_dwordx4 v161, s[20:23], s57 offen lds
  v_add_u32 v253, v240, v241
  v_mfma_f32_16x16x16_bf16 v[128:131], a[0:1], a[32:33], 0
  ds_read_b64_tr_b16 a[12:13], v3 offset:18496
  ds_read_b64_tr_b16 a[14:15], v3 offset:20800
  v_mfma_f32_16x16x16_bf16 v[144:147], a[4:5], a[32:33], 0
  ds_read_b64_tr_b16 a[16:17], v3 offset:18784
  v_lshl_add_u32 v254, v232, 3, v240
  v_mfma_f32_16x16x16_bf16 v[148:151], a[4:5], a[36:37], 0
  ds_read_b64_tr_b16 a[18:19], v3 offset:21088
  v_add_u32 v254, 256, v254
  v_mfma_f32_16x16x16_bf16 v[156:159], a[8:9], a[36:37], 0
  ds_read_b64_tr_b16 a[20:21], v3 offset:18528
  v_mov_b32 v14, v253
  v_mfma_f32_16x16x16_bf16 v[152:155], a[8:9], a[32:33], 0
  s_add_u32 m0, s41, 73984
  s_add_u32 s45, s40, 82944
  s_waitcnt lgkmcnt(3)
  v_mfma_f32_16x16x32_bf16 v[16:19], a[12:15], a[48:51], v[16:19]
  buffer_load_dwordx4 v2, s[16:19], s45 offen lds
  v_mov_b32 v15, v254
  v_mfma_f32_16x16x32_bf16 v[20:23], a[12:15], a[52:55], v[20:23]
  ds_read_b64_tr_b16 a[22:23], v3 offset:20832
  v_and_b32 v241, 1, v235
  v_mfma_f32_16x16x32_bf16 v[24:27], a[12:15], a[56:59], v[24:27]
  ds_read_b64_tr_b16 a[24:25], v3 offset:18816
  v_lshl_add_u32 v253, v241, 5, v253
  v_mfma_f32_16x16x32_bf16 v[28:31], a[12:15], a[60:63], v[28:31]
  buffer_load_dwordx4 v2, s[16:19], s45 offen offset:1024 lds
  s_waitcnt lgkmcnt(3)
  v_mfma_f32_16x16x32_bf16 v[44:47], a[16:19], a[60:63], v[44:47]
  ds_read_b64_tr_b16 a[26:27], v3 offset:21120
  v_lshlrev_b32 v241, 8, v241
  v_mfma_f32_16x16x32_bf16 v[40:43], a[16:19], a[56:59], v[40:43]
  ds_read_b64_tr_b16 a[0:1], v3 offset:18560
  v_sub_u32 v254, v254, v241
  v_mfma_f32_16x16x32_bf16 v[36:39], a[16:19], a[52:55], v[36:39]
  buffer_load_dwordx4 v2, s[16:19], s45 offen offset:2048 lds
  v_and_b32 v241, 1, v235
  v_mfma_f32_16x16x32_bf16 v[32:35], a[16:19], a[48:51], v[32:35]
  ds_read_b64_tr_b16 a[2:3], v3 offset:20864
  s_waitcnt lgkmcnt(4)
  v_mfma_f32_16x16x32_bf16 v[48:51], a[20:23], a[48:51], v[48:51]
  ds_read_b64_tr_b16 a[4:5], v3 offset:18848
  v_cmp_ne_u32 vcc, 0, v241
  v_mfma_f32_16x16x32_bf16 v[52:55], a[20:23], a[52:55], v[52:55]
  buffer_load_dwordx4 v2, s[16:19], s45 offen offset:3072 lds
  s_mov_b32 s48, 4608
  v_mfma_f32_16x16x32_bf16 v[56:59], a[20:23], a[56:59], v[56:59]
  ds_read_b64_tr_b16 a[6:7], v3 offset:21152
  s_mul_i32 s49, s15, 10368
  v_mfma_f32_16x16x32_bf16 v[60:63], a[20:23], a[60:63], v[60:63]
  ds_read_b64_tr_b16 a[8:9], v3 offset:18592
  s_waitcnt lgkmcnt(4)
  v_mfma_f32_16x16x32_bf16 v[76:79], a[24:27], a[60:63], v[76:79]
  buffer_load_dwordx4 v2, s[16:19], s45 offen offset:3584 lds
  s_add_u32 s50, s49, 110976
  v_mfma_f32_16x16x32_bf16 v[72:75], a[24:27], a[56:59], v[72:75]
  ds_read_b64_tr_b16 a[10:11], v3 offset:20896
  s_and_b32 s36, s55, 31
  v_mfma_f32_16x16x32_bf16 v[68:71], a[24:27], a[52:55], v[68:71]
  ds_read_b64_tr_b16 a[12:13], v3 offset:18880
  s_mul_i32 s36, s36, 1408
  v_mfma_f32_16x16x32_bf16 v[64:67], a[24:27], a[48:51], v[64:67]
  s_add_u32 m0, s41, 92480
  s_add_u32 s45, s40, 165888
  s_waitcnt lgkmcnt(4)
  v_mfma_f32_16x16x32_bf16 v[80:83], a[0:3], a[48:51], v[80:83]
  buffer_load_dwordx4 v2, s[16:19], s45 offen lds
  v_lshrrev_b32 v163, 4, v0
  v_mfma_f32_16x16x32_bf16 v[84:87], a[0:3], a[52:55], v[84:87]
  ds_read_b64_tr_b16 a[14:15], v3 offset:21184
  v_lshlrev_b32 v163, 20, v163
  v_mfma_f32_16x16x32_bf16 v[88:91], a[0:3], a[56:59], v[88:91]
  ds_read_b64_tr_b16 a[16:17], v5 offset:18624
  v_lshl_add_u32 v162, v0, 7, v163
  v_mfma_f32_16x16x32_bf16 v[92:95], a[0:3], a[60:63], v[92:95]
  buffer_load_dwordx4 v2, s[16:19], s45 offen offset:1024 lds
  s_waitcnt lgkmcnt(3)
  v_mfma_f32_16x16x32_bf16 v[108:111], a[4:7], a[60:63], v[108:111]
  ds_read_b64_tr_b16 a[18:19], v5 offset:20928
  v_add_u32 v162, s36, v162
  v_mfma_f32_16x16x32_bf16 v[104:107], a[4:7], a[56:59], v[104:107]
  ds_read_b64_tr_b16 a[20:21], v5 offset:18912
  v_mfma_f32_16x16x32_bf16 v[100:103], a[4:7], a[52:55], v[100:103]
  buffer_load_dwordx4 v2, s[16:19], s45 offen offset:2048 lds
  v_mfma_f32_16x16x32_bf16 v[96:99], a[4:7], a[48:51], v[96:99]
  s_waitcnt vmcnt(23)
  s_barrier
  v_mfma_f32_16x16x32_bf16 v[112:115], a[8:11], a[48:51], v[112:115]
  ds_read_b128 a[32:35], v8 offset:128
  v_mfma_f32_16x16x32_bf16 v[116:119], a[8:11], a[52:55], v[116:119]
  buffer_load_dwordx4 v2, s[16:19], s45 offen offset:3072 lds
  v_mfma_f32_16x16x32_bf16 v[120:123], a[8:11], a[56:59], v[120:123]
  ds_read_b128 a[36:39], v8 offset:640
  v_mfma_f32_16x16x32_bf16 v[124:127], a[8:11], a[60:63], v[124:127]
  ds_read_b128 a[40:43], v10 offset:128
  s_waitcnt lgkmcnt(3)
  v_mfma_f32_16x16x32_bf16 v[140:143], a[12:15], a[60:63], v[140:143]
  buffer_load_dwordx4 v2, s[16:19], s45 offen offset:3584 lds
  ds_read_b128 a[44:47], v10 offset:640
  v_mfma_f32_16x16x32_bf16 v[136:139], a[12:15], a[56:59], v[136:139]
  ds_read_b64_tr_b16 a[22:23], v5 offset:21216
  v_mfma_f32_16x16x32_bf16 v[132:135], a[12:15], a[52:55], v[132:135]
  ds_read_b64_tr_b16 a[24:25], v3 offset:36992
  v_mfma_f32_16x16x32_bf16 v[128:131], a[12:15], a[48:51], v[128:131]
  ds_read_b64_tr_b16 a[26:27], v3 offset:39296
  v_mfma_f32_16x16x32_bf16 v[144:147], a[16:19], a[48:51], v[144:147]
  ds_read_b64_tr_b16 a[0:1], v3 offset:37280
  v_mfma_f32_16x16x32_bf16 v[148:151], a[16:19], a[52:55], v[148:151]
  ds_read_b64_tr_b16 a[2:3], v3 offset:39584
  s_waitcnt lgkmcnt(4)
  v_mfma_f32_16x16x32_bf16 v[156:159], a[20:23], a[52:55], v[156:159]
  ds_read_b64_tr_b16 a[4:5], v3 offset:37024
  v_mfma_f32_16x16x32_bf16 v[152:155], a[20:23], a[48:51], v[152:155]
  s_add_u32 m0, s41, 0
  s_add_u32 s45, s40, 248832
  s_waitcnt lgkmcnt(3)
  v_mfma_f32_16x16x32_bf16 v[16:19], a[24:27], a[32:35], v[16:19]
  buffer_load_dwordx4 v2, s[16:19], s45 offen lds
  v_mfma_f32_16x16x32_bf16 v[20:23], a[24:27], a[36:39], v[20:23]
  ds_read_b64_tr_b16 a[6:7], v3 offset:39328
  v_mfma_f32_16x16x32_bf16 v[24:27], a[24:27], a[40:43], v[24:27]
  ds_read_b64_tr_b16 a[8:9], v3 offset:37312
  v_mfma_f32_16x16x32_bf16 v[28:31], a[24:27], a[44:47], v[28:31]
  buffer_load_dwordx4 v2, s[16:19], s45 offen offset:1024 lds
  s_waitcnt lgkmcnt(3)
  v_mfma_f32_16x16x32_bf16 v[44:47], a[0:3], a[44:47], v[44:47]
  ds_read_b64_tr_b16 a[10:11], v3 offset:39616
  v_mfma_f32_16x16x32_bf16 v[40:43], a[0:3], a[40:43], v[40:43]
  ds_read_b64_tr_b16 a[12:13], v3 offset:37056
  v_mfma_f32_16x16x32_bf16 v[36:39], a[0:3], a[36:39], v[36:39]
  buffer_load_dwordx4 v2, s[16:19], s45 offen offset:2048 lds
  v_mfma_f32_16x16x32_bf16 v[32:35], a[0:3], a[32:35], v[32:35]
  ds_read_b64_tr_b16 a[14:15], v3 offset:39360
  s_waitcnt lgkmcnt(4)
  v_mfma_f32_16x16x32_bf16 v[48:51], a[4:7], a[32:35], v[48:51]
  ds_read_b64_tr_b16 a[16:17], v3 offset:37344
  v_mfma_f32_16x16x32_bf16 v[52:55], a[4:7], a[36:39], v[52:55]
  buffer_load_dwordx4 v2, s[16:19], s45 offen offset:3072 lds
  v_mfma_f32_16x16x32_bf16 v[56:59], a[4:7], a[40:43], v[56:59]
  ds_read_b64_tr_b16 a[18:19], v3 offset:39648
  v_mfma_f32_16x16x32_bf16 v[60:63], a[4:7], a[44:47], v[60:63]
  ds_read_b64_tr_b16 a[20:21], v3 offset:37088
  s_waitcnt lgkmcnt(4)
  v_mfma_f32_16x16x32_bf16 v[76:79], a[8:11], a[44:47], v[76:79]
  buffer_load_dwordx4 v2, s[16:19], s45 offen offset:3584 lds
  v_mfma_f32_16x16x32_bf16 v[72:75], a[8:11], a[40:43], v[72:75]
  ds_read_b64_tr_b16 a[22:23], v3 offset:39392
  v_mfma_f32_16x16x32_bf16 v[68:71], a[8:11], a[36:39], v[68:71]
  ds_read_b64_tr_b16 a[24:25], v3 offset:37376
  v_mfma_f32_16x16x32_bf16 v[64:67], a[8:11], a[32:35], v[64:67]
  ds_read_b64_tr_b16 a[26:27], v3 offset:39680
  s_waitcnt lgkmcnt(5)
  v_mfma_f32_16x16x32_bf16 v[80:83], a[12:15], a[32:35], v[80:83]
  ds_read_b64_tr_b16 a[0:1], v5 offset:37120
  v_mfma_f32_16x16x32_bf16 v[84:87], a[12:15], a[36:39], v[84:87]
  ds_read_b64_tr_b16 a[2:3], v5 offset:39424
  v_mfma_f32_16x16x32_bf16 v[88:91], a[12:15], a[40:43], v[88:91]
  ds_read_b64_tr_b16 a[4:5], v5 offset:37408
  v_mfma_f32_16x16x32_bf16 v[92:95], a[12:15], a[44:47], v[92:95]
  ds_read_b64_tr_b16 a[6:7], v5 offset:39712
  s_waitcnt lgkmcnt(6)
  v_mfma_f32_16x16x32_bf16 v[108:111], a[16:19], a[44:47], v[108:111]
  v_mfma_f32_16x16x32_bf16 v[104:107], a[16:19], a[40:43], v[104:107]
  v_mfma_f32_16x16x32_bf16 v[100:103], a[16:19], a[36:39], v[100:103]
  v_mfma_f32_16x16x32_bf16 v[96:99], a[16:19], a[32:35], v[96:99]
  s_waitcnt vmcnt(15)
  s_barrier
  v_mfma_f32_16x16x32_bf16 v[112:115], a[20:23], a[32:35], v[112:115]
  s_add_u32 m0, s41, 18496
  s_add_u32 s45, s40, 331776
  v_mfma_f32_16x16x32_bf16 v[116:119], a[20:23], a[36:39], v[116:119]
  buffer_load_dwordx4 v2, s[16:19], s45 offen lds
  ds_read_b128 a[48:51], v7 offset:2304
  v_mfma_f32_16x16x32_bf16 v[120:123], a[20:23], a[40:43], v[120:123]
  ds_read_b128 a[52:55], v7 offset:6912
  v_mfma_f32_16x16x32_bf16 v[124:127], a[20:23], a[44:47], v[124:127]
  buffer_load_dwordx4 v2, s[16:19], s45 offen offset:1024 lds
  s_waitcnt lgkmcnt(2)
  v_mfma_f32_16x16x32_bf16 v[140:143], a[24:27], a[44:47], v[140:143]
  ds_read_b128 a[56:59], v9 offset:2304
  ds_read_b128 a[60:63], v9 offset:6912
  v_mfma_f32_16x16x32_bf16 v[136:139], a[24:27], a[40:43], v[136:139]
  ds_read_b64_tr_b16 a[8:9], v3 offset:55488
  v_mfma_f32_16x16x32_bf16 v[132:135], a[24:27], a[36:39], v[132:135]
  buffer_load_dwordx4 v2, s[16:19], s45 offen offset:2048 lds
  v_mfma_f32_16x16x32_bf16 v[128:131], a[24:27], a[32:35], v[128:131]
  ds_read_b64_tr_b16 a[10:11], v3 offset:57792
  v_mfma_f32_16x16x32_bf16 v[144:147], a[0:3], a[32:35], v[144:147]
  ds_read_b64_tr_b16 a[12:13], v3 offset:55776
  v_mfma_f32_16x16x32_bf16 v[148:151], a[0:3], a[36:39], v[148:151]
  buffer_load_dwordx4 v2, s[16:19], s45 offen offset:3072 lds
  v_mfma_f32_16x16x32_bf16 v[156:159], a[4:7], a[36:39], v[156:159]
  ds_read_b64_tr_b16 a[14:15], v3 offset:58080
  v_mfma_f32_16x16x32_bf16 v[152:155], a[4:7], a[32:35], v[152:155]
  ds_read_b64_tr_b16 a[16:17], v3 offset:55520
  s_waitcnt lgkmcnt(3)
  v_mfma_f32_16x16x32_bf16 v[16:19], a[8:11], a[48:51], v[16:19]
  buffer_load_dwordx4 v2, s[16:19], s45 offen offset:3584 lds
  v_mfma_f32_16x16x32_bf16 v[20:23], a[8:11], a[52:55], v[20:23]
  ds_read_b64_tr_b16 a[18:19], v3 offset:57824
  v_mfma_f32_16x16x32_bf16 v[24:27], a[8:11], a[56:59], v[24:27]
  ds_read_b64_tr_b16 a[20:21], v3 offset:55808
  v_mfma_f32_16x16x32_bf16 v[28:31], a[8:11], a[60:63], v[28:31]
  ds_read_b64_tr_b16 a[22:23], v3 offset:58112
  s_waitcnt lgkmcnt(4)
  v_mfma_f32_16x16x32_bf16 v[44:47], a[12:15], a[60:63], v[44:47]
  ds_read_b64_tr_b16 a[24:25], v3 offset:55552
  v_mfma_f32_16x16x32_bf16 v[40:43], a[12:15], a[56:59], v[40:43]
  ds_read_b64_tr_b16 a[26:27], v3 offset:57856
  v_mfma_f32_16x16x32_bf16 v[36:39], a[12:15], a[52:55], v[36:39]
  ds_read_b64_tr_b16 a[0:1], v3 offset:55840
  v_mfma_f32_16x16x32_bf16 v[32:35], a[12:15], a[48:51], v[32:35]
  ds_read_b64_tr_b16 a[2:3], v3 offset:58144
  s_waitcnt lgkmcnt(6)
  v_mfma_f32_16x16x32_bf16 v[48:51], a[16:19], a[48:51], v[48:51]
  ds_read_b64_tr_b16 a[4:5], v3 offset:55584
  v_mfma_f32_16x16x32_bf16 v[52:55], a[16:19], a[52:55], v[52:55]
  ds_read_b64_tr_b16 a[6:7], v3 offset:57888
  v_mfma_f32_16x16x32_bf16 v[56:59], a[16:19], a[56:59], v[56:59]
  ds_read_b64_tr_b16 a[8:9], v3 offset:55872
  v_mfma_f32_16x16x32_bf16 v[60:63], a[16:19], a[60:63], v[60:63]
  s_waitcnt vmcnt(15)
  s_barrier
  s_waitcnt lgkmcnt(5)
  v_mfma_f32_16x16x32_bf16 v[76:79], a[20:23], a[60:63], v[76:79]
  s_add_u32 m0, s41, 36992
  s_add_u32 s45, s40, 18432
  v_mfma_f32_16x16x32_bf16 v[72:75], a[20:23], a[56:59], v[72:75]
  buffer_load_dwordx4 v2, s[16:19], s45 offen lds
  v_mfma_f32_16x16x32_bf16 v[68:71], a[20:23], a[52:55], v[68:71]
  ds_read_b64_tr_b16 a[10:11], v3 offset:58176
  v_mfma_f32_16x16x32_bf16 v[64:67], a[20:23], a[48:51], v[64:67]
  ds_read_b128 a[32:35], v7 offset:1728
  v_mfma_f32_16x16x32_bf16 v[80:83], a[24:27], a[48:51], v[80:83]
  ds_read_b128 a[36:39], v7 offset:6336
  v_mfma_f32_16x16x32_bf16 v[84:87], a[24:27], a[52:55], v[84:87]
  ds_read_b64_tr_b16 a[12:13], v5 offset:55616
  v_mfma_f32_16x16x32_bf16 v[88:91], a[24:27], a[56:59], v[88:91]
  buffer_load_dwordx4 v2, s[16:19], s45 offen offset:1024 lds
  v_mfma_f32_16x16x32_bf16 v[92:95], a[24:27], a[60:63], v[92:95]
  ds_read_b64_tr_b16 a[14:15], v5 offset:57920
  s_waitcnt lgkmcnt(5)
  v_mfma_f32_16x16x32_bf16 v[108:111], a[0:3], a[60:63], v[108:111]
  ds_read_b128 a[40:43], v9 offset:1728
  v_mfma_f32_16x16x32_bf16 v[104:107], a[0:3], a[56:59], v[104:107]
  ds_read_b128 a[44:47], v9 offset:6336
  v_mfma_f32_16x16x32_bf16 v[100:103], a[0:3], a[52:55], v[100:103]
  ds_read_b64_tr_b16 a[16:17], v5 offset:55904
  v_mfma_f32_16x16x32_bf16 v[96:99], a[0:3], a[48:51], v[96:99]
  ds_read_b64_tr_b16 a[18:19], v5 offset:58208
  v_mfma_f32_16x16x32_bf16 v[112:115], a[4:7], a[48:51], v[112:115]
  buffer_load_dwordx4 v2, s[16:19], s45 offen offset:2048 lds
  v_mfma_f32_16x16x32_bf16 v[116:119], a[4:7], a[52:55], v[116:119]
  ds_read_b64_tr_b16 a[20:21], v4
  v_mfma_f32_16x16x32_bf16 v[120:123], a[4:7], a[56:59], v[120:123]
  ds_read_b64_tr_b16 a[22:23], v4 offset:2304
  v_mfma_f32_16x16x32_bf16 v[124:127], a[4:7], a[60:63], v[124:127]
  ds_read_b64_tr_b16 a[24:25], v4 offset:288
  s_waitcnt lgkmcnt(5)
  v_mfma_f32_16x16x32_bf16 v[140:143], a[8:11], a[60:63], v[140:143]
  ds_read_b64_tr_b16 a[26:27], v4 offset:2592
  v_mfma_f32_16x16x32_bf16 v[136:139], a[8:11], a[56:59], v[136:139]
  ds_read_b64_tr_b16 a[0:1], v4 offset:32
  v_mfma_f32_16x16x32_bf16 v[132:135], a[8:11], a[52:55], v[132:135]
  buffer_load_dwordx4 v2, s[16:19], s45 offen offset:3072 lds
  v_mfma_f32_16x16x32_bf16 v[128:131], a[8:11], a[48:51], v[128:131]
  ds_read_b64_tr_b16 a[2:3], v4 offset:2336
  v_mfma_f32_16x16x32_bf16 v[144:147], a[12:15], a[48:51], v[144:147]
  ds_read_b64_tr_b16 a[4:5], v4 offset:320
  v_mfma_f32_16x16x32_bf16 v[148:151], a[12:15], a[52:55], v[148:151]
  ds_read_b64_tr_b16 a[6:7], v4 offset:2624
  s_waitcnt lgkmcnt(5)
  v_mfma_f32_16x16x32_bf16 v[156:159], a[16:19], a[52:55], v[156:159]
  ds_read_b64_tr_b16 a[8:9], v4 offset:64
  v_mfma_f32_16x16x32_bf16 v[152:155], a[16:19], a[48:51], v[152:155]
  ds_read_b64_tr_b16 a[10:11], v4 offset:2368
  v_mfma_f32_16x16x32_bf16 v[16:19], a[20:23], a[32:35], v[16:19]
  buffer_load_dwordx4 v2, s[16:19], s45 offen offset:3584 lds
  v_mfma_f32_16x16x32_bf16 v[20:23], a[20:23], a[36:39], v[20:23]
  ds_read_b64_tr_b16 a[12:13], v4 offset:352
  v_mfma_f32_16x16x32_bf16 v[24:27], a[20:23], a[40:43], v[24:27]
  s_waitcnt vmcnt(15)
  s_barrier
  v_mfma_f32_16x16x32_bf16 v[28:31], a[20:23], a[44:47], v[28:31]
  s_add_u32 m0, s41, 55488
  s_add_u32 s45, s40, 101376
  s_waitcnt lgkmcnt(3)
  v_mfma_f32_16x16x32_bf16 v[44:47], a[24:27], a[44:47], v[44:47]
  buffer_load_dwordx4 v2, s[16:19], s45 offen lds
  v_mfma_f32_16x16x32_bf16 v[40:43], a[24:27], a[40:43], v[40:43]
  ds_read_b64_tr_b16 a[14:15], v4 offset:2656
  v_mfma_f32_16x16x32_bf16 v[36:39], a[24:27], a[36:39], v[36:39]
  ds_read_b64_tr_b16 a[16:17], v4 offset:96
  v_mfma_f32_16x16x32_bf16 v[32:35], a[24:27], a[32:35], v[32:35]
  ds_read_b64_tr_b16 a[18:19], v4 offset:2400
  v_mfma_f32_16x16x32_bf16 v[48:51], a[0:3], a[32:35], v[48:51]
  ds_read_b64_tr_b16 a[20:21], v4 offset:384
  v_mfma_f32_16x16x32_bf16 v[52:55], a[0:3], a[36:39], v[52:55]
  buffer_load_dwordx4 v2, s[16:19], s45 offen offset:1024 lds
  v_mfma_f32_16x16x32_bf16 v[56:59], a[0:3], a[40:43], v[56:59]
  ds_read_b64_tr_b16 a[22:23], v4 offset:2688
  v_mfma_f32_16x16x32_bf16 v[60:63], a[0:3], a[44:47], v[60:63]
  ds_read_b128 a[48:51], v7 offset:1152
  v_mfma_f32_16x16x32_bf16 v[76:79], a[4:7], a[44:47], v[76:79]
  ds_read_b128 a[52:55], v7 offset:5760
  v_mfma_f32_16x16x32_bf16 v[72:75], a[4:7], a[40:43], v[72:75]
  ds_read_b64_tr_b16 a[24:25], v6 offset:128
  v_mfma_f32_16x16x32_bf16 v[68:71], a[4:7], a[36:39], v[68:71]
  ds_read_b64_tr_b16 a[26:27], v6 offset:2432
  v_mfma_f32_16x16x32_bf16 v[64:67], a[4:7], a[32:35], v[64:67]
  buffer_load_dwordx4 v2, s[16:19], s45 offen offset:2048 lds
  s_waitcnt lgkmcnt(5)
  v_mfma_f32_16x16x32_bf16 v[80:83], a[8:11], a[32:35], v[80:83]
  ds_read_b128 a[56:59], v9 offset:1152
  v_mfma_f32_16x16x32_bf16 v[84:87], a[8:11], a[36:39], v[84:87]
  ds_read_b128 a[60:63], v9 offset:5760
  v_mfma_f32_16x16x32_bf16 v[88:91], a[8:11], a[40:43], v[88:91]
  ds_read_b64_tr_b16 a[0:1], v6 offset:416
  v_mfma_f32_16x16x32_bf16 v[92:95], a[8:11], a[44:47], v[92:95]
  ds_read_b64_tr_b16 a[2:3], v6 offset:2720
  v_mfma_f32_16x16x32_bf16 v[108:111], a[12:15], a[44:47], v[108:111]
  ds_read_b64_tr_b16 a[4:5], v4 offset:18496
  v_mfma_f32_16x16x32_bf16 v[104:107], a[12:15], a[40:43], v[104:107]
  buffer_load_dwordx4 v2, s[16:19], s45 offen offset:3072 lds
  v_mfma_f32_16x16x32_bf16 v[100:103], a[12:15], a[36:39], v[100:103]
  ds_read_b64_tr_b16 a[6:7], v4 offset:20800
  v_mfma_f32_16x16x32_bf16 v[96:99], a[12:15], a[32:35], v[96:99]
  ds_read_b64_tr_b16 a[8:9], v4 offset:18784
  v_mfma_f32_16x16x32_bf16 v[112:115], a[16:19], a[32:35], v[112:115]
  ds_read_b64_tr_b16 a[10:11], v4 offset:21088
  v_mfma_f32_16x16x32_bf16 v[116:119], a[16:19], a[36:39], v[116:119]
  ds_read_b128 a[64:67], v7 offset:1920
  v_mfma_f32_16x16x32_bf16 v[120:123], a[16:19], a[40:43], v[120:123]
  ds_read_b64_tr_b16 a[12:13], v4 offset:18528
  v_mfma_f32_16x16x32_bf16 v[124:127], a[16:19], a[44:47], v[124:127]
  buffer_load_dwordx4 v2, s[16:19], s45 offen offset:3584 lds
  s_waitcnt lgkmcnt(5)
  v_mfma_f32_16x16x32_bf16 v[140:143], a[20:23], a[44:47], v[140:143]
  ds_read_b64_tr_b16 a[14:15], v4 offset:20832
  v_mfma_f32_16x16x32_bf16 v[136:139], a[20:23], a[40:43], v[136:139]
  ds_read_b128 a[68:71], v7 offset:6528
  v_mfma_f32_16x16x32_bf16 v[132:135], a[20:23], a[36:39], v[132:135]
  ds_read_b64_tr_b16 a[16:17], v4 offset:18816
  v_mfma_f32_16x16x32_bf16 v[128:131], a[20:23], a[32:35], v[128:131]
  ds_read_b64_tr_b16 a[18:19], v4 offset:21120
  v_mfma_f32_16x16x32_bf16 v[144:147], a[24:27], a[32:35], v[144:147]
  ds_read_b64_tr_b16 a[20:21], v4 offset:18560
  v_mfma_f32_16x16x32_bf16 v[148:151], a[24:27], a[36:39], v[148:151]
  ds_read_b64_tr_b16 a[22:23], v4 offset:20864
  v_mfma_f32_16x16x32_bf16 v[156:159], a[0:3], a[36:39], v[156:159]
  v_mfma_f32_16x16x32_bf16 v[152:155], a[0:3], a[32:35], v[152:155]
  ds_read_b64_tr_b16 a[24:25], v4 offset:18848
  s_waitcnt lgkmcnt(5)
  v_mfma_f32_16x16x32_bf16 v[16:19], a[4:7], a[48:51], v[16:19]
  ds_read_b64_tr_b16 a[26:27], v4 offset:21152
  v_mfma_f32_16x16x32_bf16 v[20:23], a[4:7], a[52:55], v[20:23]
  v_mfma_f32_16x16x32_bf16 v[24:27], a[4:7], a[56:59], v[24:27]
  s_waitcnt vmcnt(15)
  s_barrier
  v_mfma_f32_16x16x32_bf16 v[28:31], a[4:7], a[60:63], v[28:31]
  s_add_u32 m0, s41, 73984
  s_add_u32 s45, s40, 184320
  v_mfma_f32_16x16x32_bf16 v[44:47], a[8:11], a[60:63], v[44:47]
  buffer_load_dwordx4 v2, s[16:19], s45 offen lds
  v_mfma_f32_16x16x32_bf16 v[40:43], a[8:11], a[56:59], v[40:43]
  ds_read_b64_tr_b16 a[0:1], v4 offset:18592
  v_mfma_f32_16x16x32_bf16 v[36:39], a[8:11], a[52:55], v[36:39]
  ds_read_b64_tr_b16 a[2:3], v4 offset:20896
  v_mfma_f32_16x16x32_bf16 v[32:35], a[8:11], a[48:51], v[32:35]
  ds_read_b64_tr_b16 a[4:5], v4 offset:18880
  v_mfma_f32_16x16x32_bf16 v[48:51], a[12:15], a[48:51], v[48:51]
  ds_read_b64_tr_b16 a[6:7], v4 offset:21184
  v_mfma_f32_16x16x32_bf16 v[52:55], a[12:15], a[52:55], v[52:55]
  buffer_load_dwordx4 v2, s[16:19], s45 offen offset:1024 lds
  v_mfma_f32_16x16x32_bf16 v[56:59], a[12:15], a[56:59], v[56:59]
  ds_read_b128 a[32:35], v7 offset:576
  v_mfma_f32_16x16x32_bf16 v[60:63], a[12:15], a[60:63], v[60:63]
  ds_read_b128 a[36:39], v7 offset:5184
  s_waitcnt lgkmcnt(5)
  v_mfma_f32_16x16x32_bf16 v[76:79], a[16:19], a[60:63], v[76:79]
  ds_read_b64_tr_b16 a[8:9], v6 offset:18624
  v_mfma_f32_16x16x32_bf16 v[72:75], a[16:19], a[56:59], v[72:75]
  ds_read_b64_tr_b16 a[10:11], v6 offset:20928
  v_mfma_f32_16x16x32_bf16 v[68:71], a[16:19], a[52:55], v[68:71]
  ds_read_b128 a[40:43], v9 offset:576
  v_mfma_f32_16x16x32_bf16 v[64:67], a[16:19], a[48:51], v[64:67]
  buffer_load_dwordx4 v2, s[16:19], s45 offen offset:2048 lds
  v_mfma_f32_16x16x32_bf16 v[80:83], a[20:23], a[48:51], v[80:83]
  ds_read_b128 a[44:47], v9 offset:5184
  v_mfma_f32_16x16x32_bf16 v[84:87], a[20:23], a[52:55], v[84:87]
  ds_read_b64_tr_b16 a[12:13], v6 offset:18912
  v_mfma_f32_16x16x32_bf16 v[88:91], a[20:23], a[56:59], v[88:91]
  ds_read_b64_tr_b16 a[14:15], v6 offset:21216
  v_mfma_f32_16x16x32_bf16 v[92:95], a[20:23], a[60:63], v[92:95]
  ds_read_b64_tr_b16 a[16:17], v3
  v_mfma_f32_16x16x32_bf16 v[108:111], a[24:27], a[60:63], v[108:111]
  ds_read_b64_tr_b16 a[18:19], v3 offset:2304
  v_mfma_f32_16x16x32_bf16 v[104:107], a[24:27], a[56:59], v[104:107]
  buffer_load_dwordx4 v2, s[16:19], s45 offen offset:3072 lds
  v_mfma_f32_16x16x32_bf16 v[100:103], a[24:27], a[52:55], v[100:103]
  ds_read_b64_tr_b16 a[20:21], v3 offset:288
  v_mfma_f32_16x16x32_bf16 v[96:99], a[24:27], a[48:51], v[96:99]
  ds_read_b64_tr_b16 a[22:23], v3 offset:2592
  s_waitcnt lgkmcnt(5)
  v_mfma_f32_16x16x32_bf16 v[112:115], a[0:3], a[48:51], v[112:115]
  ds_read_b128 a[72:75], v9 offset:1920
  v_mfma_f32_16x16x32_bf16 v[116:119], a[0:3], a[52:55], v[116:119]
  ds_read_b64_tr_b16 a[24:25], v3 offset:32
  v_mfma_f32_16x16x32_bf16 v[120:123], a[0:3], a[56:59], v[120:123]
  ds_read_b64_tr_b16 a[26:27], v3 offset:2336
  v_mfma_f32_16x16x32_bf16 v[124:127], a[0:3], a[60:63], v[124:127]
  buffer_load_dwordx4 v2, s[16:19], s45 offen offset:3584 lds
  v_mfma_f32_16x16x32_bf16 v[140:143], a[4:7], a[60:63], v[140:143]
  ds_read_b128 a[76:79], v9 offset:6528
  v_mfma_f32_16x16x32_bf16 v[136:139], a[4:7], a[56:59], v[136:139]
  ds_read_b64_tr_b16 a[0:1], v3 offset:320
  v_mfma_f32_16x16x32_bf16 v[132:135], a[4:7], a[52:55], v[132:135]
  ds_read_b64_tr_b16 a[2:3], v3 offset:2624
  v_mfma_f32_16x16x32_bf16 v[128:131], a[4:7], a[48:51], v[128:131]
  ds_read_b64_tr_b16 a[4:5], v3 offset:64
  v_mfma_f32_16x16x32_bf16 v[144:147], a[8:11], a[48:51], v[144:147]
  ds_read_b64_tr_b16 a[6:7], v3 offset:2368
  v_mfma_f32_16x16x32_bf16 v[148:151], a[8:11], a[52:55], v[148:151]
  s_waitcnt lgkmcnt(5)
  v_mfma_f32_16x16x32_bf16 v[156:159], a[12:15], a[52:55], v[156:159]
  v_mfma_f32_16x16x32_bf16 v[152:155], a[12:15], a[48:51], v[152:155]
  ds_read_b64_tr_b16 a[8:9], v3 offset:352
  v_mfma_f32_16x16x32_bf16 v[16:19], a[16:19], a[32:35], v[16:19]
  ds_read_b64_tr_b16 a[10:11], v3 offset:2656
  v_mfma_f32_16x16x32_bf16 v[20:23], a[16:19], a[36:39], v[20:23]
  v_mfma_f32_16x16x32_bf16 v[24:27], a[16:19], a[40:43], v[24:27]
  s_waitcnt vmcnt(15)
  s_barrier
  v_mfma_f32_16x16x32_bf16 v[28:31], a[16:19], a[44:47], v[28:31]
  s_add_u32 m0, s41, 92480
  s_add_u32 s45, s40, 267264
  v_mfma_f32_16x16x32_bf16 v[44:47], a[20:23], a[44:47], v[44:47]
  buffer_load_dwordx4 v2, s[16:19], s45 offen lds
  v_mfma_f32_16x16x32_bf16 v[40:43], a[20:23], a[40:43], v[40:43]
  ds_read_b64_tr_b16 a[12:13], v3 offset:96
  v_mfma_f32_16x16x32_bf16 v[36:39], a[20:23], a[36:39], v[36:39]
  ds_read_b64_tr_b16 a[14:15], v3 offset:2400
  v_mfma_f32_16x16x32_bf16 v[32:35], a[20:23], a[32:35], v[32:35]
  ds_read_b64_tr_b16 a[16:17], v3 offset:384
  v_mfma_f32_16x16x32_bf16 v[48:51], a[24:27], a[32:35], v[48:51]
  ds_read_b64_tr_b16 a[18:19], v3 offset:2688
  v_mfma_f32_16x16x32_bf16 v[52:55], a[24:27], a[36:39], v[52:55]
  buffer_load_dwordx4 v2, s[16:19], s45 offen offset:1024 lds
  v_mfma_f32_16x16x32_bf16 v[56:59], a[24:27], a[40:43], v[56:59]
  ds_read_b128 a[48:51], v7
  v_mfma_f32_16x16x32_bf16 v[60:63], a[24:27], a[44:47], v[60:63]
  ds_read_b128 a[52:55], v7 offset:4608
  s_waitcnt lgkmcnt(5)
  v_mfma_f32_16x16x32_bf16 v[76:79], a[0:3], a[44:47], v[76:79]
  ds_read_b64_tr_b16 a[20:21], v5 offset:128
  v_mfma_f32_16x16x32_bf16 v[72:75], a[0:3], a[40:43], v[72:75]
  ds_read_b64_tr_b16 a[22:23], v5 offset:2432
  v_mfma_f32_16x16x32_bf16 v[68:71], a[0:3], a[36:39], v[68:71]
  ds_read_b128 a[56:59], v9
  v_mfma_f32_16x16x32_bf16 v[64:67], a[0:3], a[32:35], v[64:67]
  buffer_load_dwordx4 v2, s[16:19], s45 offen offset:2048 lds
  v_mfma_f32_16x16x32_bf16 v[80:83], a[4:7], a[32:35], v[80:83]
  ds_read_b128 a[60:63], v9 offset:4608
  v_mfma_f32_16x16x32_bf16 v[84:87], a[4:7], a[36:39], v[84:87]
  ds_read_b64_tr_b16 a[24:25], v5 offset:416
  v_mfma_f32_16x16x32_bf16 v[88:91], a[4:7], a[40:43], v[88:91]
  ds_read_b64_tr_b16 a[26:27], v5 offset:2720
  v_mfma_f32_16x16x32_bf16 v[92:95], a[4:7], a[44:47], v[92:95]
  ds_read_b64_tr_b16 a[0:1], v3 offset:18496
  v_mfma_f32_16x16x32_bf16 v[108:111], a[8:11], a[44:47], v[108:111]
  ds_read_b64_tr_b16 a[2:3], v3 offset:20800
  v_mfma_f32_16x16x32_bf16 v[104:107], a[8:11], a[40:43], v[104:107]
  buffer_load_dwordx4 v2, s[16:19], s45 offen offset:3072 lds
  v_mfma_f32_16x16x32_bf16 v[100:103], a[8:11], a[36:39], v[100:103]
  ds_read_b64_tr_b16 a[4:5], v3 offset:18784
  v_mfma_f32_16x16x32_bf16 v[96:99], a[8:11], a[32:35], v[96:99]
  ds_read_b64_tr_b16 a[6:7], v3 offset:21088
  s_waitcnt lgkmcnt(5)
  v_mfma_f32_16x16x32_bf16 v[112:115], a[12:15], a[32:35], v[112:115]
  ds_read_b128 a[80:83], v7 offset:1344
  v_mfma_f32_16x16x32_bf16 v[116:119], a[12:15], a[36:39], v[116:119]
  ds_read_b64_tr_b16 a[8:9], v3 offset:18528
  v_mfma_f32_16x16x32_bf16 v[120:123], a[12:15], a[40:43], v[120:123]
  ds_read_b64_tr_b16 a[10:11], v3 offset:20832
  v_mfma_f32_16x16x32_bf16 v[124:127], a[12:15], a[44:47], v[124:127]
  buffer_load_dwordx4 v2, s[16:19], s45 offen offset:3584 lds
  v_mfma_f32_16x16x32_bf16 v[140:143], a[16:19], a[44:47], v[140:143]
  ds_read_b128 a[84:87], v7 offset:5952
  v_mfma_f32_16x16x32_bf16 v[136:139], a[16:19], a[40:43], v[136:139]
  ds_read_b64_tr_b16 a[12:13], v3 offset:18816
  v_mfma_f32_16x16x32_bf16 v[132:135], a[16:19], a[36:39], v[132:135]
  ds_read_b64_tr_b16 a[14:15], v3 offset:21120
  v_mfma_f32_16x16x32_bf16 v[128:131], a[16:19], a[32:35], v[128:131]
  ds_read_b64_tr_b16 a[16:17], v3 offset:18560
  v_mfma_f32_16x16x32_bf16 v[144:147], a[20:23], a[32:35], v[144:147]
  ds_read_b64_tr_b16 a[18:19], v3 offset:20864
  v_mfma_f32_16x16x32_bf16 v[148:151], a[20:23], a[36:39], v[148:151]
  s_waitcnt lgkmcnt(5)
  v_mfma_f32_16x16x32_bf16 v[156:159], a[24:27], a[36:39], v[156:159]
  v_mfma_f32_16x16x32_bf16 v[152:155], a[24:27], a[32:35], v[152:155]
  ds_read_b64_tr_b16 a[20:21], v3 offset:18848
  v_mfma_f32_16x16x32_bf16 v[16:19], a[0:3], a[48:51], v[16:19]
  ds_read_b64_tr_b16 a[22:23], v3 offset:21152
  v_mfma_f32_16x16x32_bf16 v[20:23], a[0:3], a[52:55], v[20:23]
  v_mfma_f32_16x16x32_bf16 v[24:27], a[0:3], a[56:59], v[24:27]
  s_waitcnt vmcnt(15)
  s_barrier
  v_mfma_f32_16x16x32_bf16 v[28:31], a[0:3], a[60:63], v[28:31]
  s_add_u32 m0, s41, 0
  s_add_u32 s45, s40, 350208
  v_mfma_f32_16x16x32_bf16 v[44:47], a[4:7], a[60:63], v[44:47]
  buffer_load_dwordx4 v2, s[16:19], s45 offen lds
  v_mfma_f32_16x16x32_bf16 v[40:43], a[4:7], a[56:59], v[40:43]
  ds_read_b64_tr_b16 a[24:25], v3 offset:18592
  v_mfma_f32_16x16x32_bf16 v[36:39], a[4:7], a[52:55], v[36:39]
  ds_read_b64_tr_b16 a[26:27], v3 offset:20896
  v_mfma_f32_16x16x32_bf16 v[32:35], a[4:7], a[48:51], v[32:35]
  ds_read_b64_tr_b16 a[0:1], v3 offset:18880
  v_mfma_f32_16x16x32_bf16 v[48:51], a[8:11], a[48:51], v[48:51]
  ds_read_b64_tr_b16 a[2:3], v3 offset:21184
  v_mfma_f32_16x16x32_bf16 v[52:55], a[8:11], a[52:55], v[52:55]
  buffer_load_dwordx4 v2, s[16:19], s45 offen offset:1024 lds
  v_mfma_f32_16x16x32_bf16 v[56:59], a[8:11], a[56:59], v[56:59]
  ds_read_b128 a[32:35], v7 offset:2368
  v_mfma_f32_16x16x32_bf16 v[60:63], a[8:11], a[60:63], v[60:63]
  ds_read_b128 a[36:39], v7 offset:6976
  s_waitcnt lgkmcnt(5)
  v_mfma_f32_16x16x32_bf16 v[76:79], a[12:15], a[60:63], v[76:79]
  ds_read_b64_tr_b16 a[4:5], v5 offset:18624
  v_mfma_f32_16x16x32_bf16 v[72:75], a[12:15], a[56:59], v[72:75]
  ds_read_b64_tr_b16 a[6:7], v5 offset:20928
  v_mfma_f32_16x16x32_bf16 v[68:71], a[12:15], a[52:55], v[68:71]
  ds_read_b128 a[40:43], v9 offset:2368
  v_mfma_f32_16x16x32_bf16 v[64:67], a[12:15], a[48:51], v[64:67]
  buffer_load_dwordx4 v2, s[16:19], s45 offen offset:2048 lds
  v_mfma_f32_16x16x32_bf16 v[80:83], a[16:19], a[48:51], v[80:83]
  ds_read_b128 a[44:47], v9 offset:6976
  v_mfma_f32_16x16x32_bf16 v[84:87], a[16:19], a[52:55], v[84:87]
  ds_read_b64_tr_b16 a[8:9], v5 offset:18912
  v_mfma_f32_16x16x32_bf16 v[88:91], a[16:19], a[56:59], v[88:91]
  ds_read_b64_tr_b16 a[10:11], v5 offset:21216
  v_mfma_f32_16x16x32_bf16 v[92:95], a[16:19], a[60:63], v[92:95]
  ds_read_b64_tr_b16 a[12:13], v3 offset:36992
  v_mfma_f32_16x16x32_bf16 v[108:111], a[20:23], a[60:63], v[108:111]
  ds_read_b64_tr_b16 a[14:15], v3 offset:39296
  v_mfma_f32_16x16x32_bf16 v[104:107], a[20:23], a[56:59], v[104:107]
  buffer_load_dwordx4 v2, s[16:19], s45 offen offset:3072 lds
  v_mfma_f32_16x16x32_bf16 v[100:103], a[20:23], a[52:55], v[100:103]
  ds_read_b64_tr_b16 a[16:17], v3 offset:37280
  v_mfma_f32_16x16x32_bf16 v[96:99], a[20:23], a[48:51], v[96:99]
  ds_read_b64_tr_b16 a[18:19], v3 offset:39584
  s_waitcnt lgkmcnt(5)
  v_mfma_f32_16x16x32_bf16 v[112:115], a[24:27], a[48:51], v[112:115]
  ds_read_b128 a[88:91], v9 offset:1344
  v_mfma_f32_16x16x32_bf16 v[116:119], a[24:27], a[52:55], v[116:119]
  ds_read_b64_tr_b16 a[20:21], v3 offset:37024
  v_mfma_f32_16x16x32_bf16 v[120:123], a[24:27], a[56:59], v[120:123]
  ds_read_b64_tr_b16 a[22:23], v3 offset:39328
  v_mfma_f32_16x16x32_bf16 v[124:127], a[24:27], a[60:63], v[124:127]
  buffer_load_dwordx4 v2, s[16:19], s45 offen offset:3584 lds
  v_mfma_f32_16x16x32_bf16 v[140:143], a[0:3], a[60:63], v[140:143]
  ds_read_b128 a[92:95], v9 offset:5952
  v_mfma_f32_16x16x32_bf16 v[136:139], a[0:3], a[56:59], v[136:139]
  ds_read_b64_tr_b16 a[24:25], v3 offset:37312
  v_mfma_f32_16x16x32_bf16 v[132:135], a[0:3], a[52:55], v[132:135]
  ds_read_b64_tr_b16 a[26:27], v3 offset:39616
  v_mfma_f32_16x16x32_bf16 v[128:131], a[0:3], a[48:51], v[128:131]
  ds_read_b64_tr_b16 a[0:1], v3 offset:37056
  v_mfma_f32_16x16x32_bf16 v[144:147], a[4:7], a[48:51], v[144:147]
  ds_read_b64_tr_b16 a[2:3], v3 offset:39360
  v_mfma_f32_16x16x32_bf16 v[148:151], a[4:7], a[52:55], v[148:151]
  s_waitcnt lgkmcnt(5)
  v_mfma_f32_16x16x32_bf16 v[156:159], a[8:11], a[52:55], v[156:159]
  v_mfma_f32_16x16x32_bf16 v[152:155], a[8:11], a[48:51], v[152:155]
  buffer_load_dword v255, v162, s[24:27], 0 offen
  v_mfma_f32_16x16x32_bf16 v[16:19], a[12:15], a[32:35], v[16:19]
  ds_read_b64_tr_b16 a[4:5], v3 offset:37344
  v_mfma_f32_16x16x32_bf16 v[20:23], a[12:15], a[36:39], v[20:23]
  ds_read_b64_tr_b16 a[6:7], v3 offset:39648
  v_mfma_f32_16x16x32_bf16 v[24:27], a[12:15], a[40:43], v[24:27]
  s_waitcnt vmcnt(16)
  s_barrier
  v_mfma_f32_16x16x32_bf16 v[28:31], a[12:15], a[44:47], v[28:31]
  s_add_u32 m0, s41, 18496
  s_add_u32 s45, s40, 36864
  v_mfma_f32_16x16x32_bf16 v[44:47], a[16:19], a[44:47], v[44:47]
  buffer_load_dwordx4 v2, s[16:19], s45 offen lds
  v_mfma_f32_16x16x32_bf16 v[40:43], a[16:19], a[40:43], v[40:43]
  ds_read_b64_tr_b16 a[8:9], v3 offset:37088
  v_mfma_f32_16x16x32_bf16 v[36:39], a[16:19], a[36:39], v[36:39]
  ds_read_b64_tr_b16 a[10:11], v3 offset:39392
  v_mfma_f32_16x16x32_bf16 v[32:35], a[16:19], a[32:35], v[32:35]
  ds_read_b64_tr_b16 a[12:13], v3 offset:37376
  v_mfma_f32_16x16x32_bf16 v[48:51], a[20:23], a[32:35], v[48:51]
  ds_read_b64_tr_b16 a[14:15], v3 offset:39680
  v_mfma_f32_16x16x32_bf16 v[52:55], a[20:23], a[36:39], v[52:55]
  buffer_load_dwordx4 v2, s[16:19], s45 offen offset:1024 lds
  v_mfma_f32_16x16x32_bf16 v[56:59], a[20:23], a[40:43], v[56:59]
  ds_read_b128 a[48:51], v7 offset:1792
  v_mfma_f32_16x16x32_bf16 v[60:63], a[20:23], a[44:47], v[60:63]
  ds_read_b128 a[52:55], v7 offset:6400
  s_waitcnt lgkmcnt(5)
  v_mfma_f32_16x16x32_bf16 v[76:79], a[24:27], a[44:47], v[76:79]
  ds_read_b64_tr_b16 a[16:17], v5 offset:37120
  v_mfma_f32_16x16x32_bf16 v[72:75], a[24:27], a[40:43], v[72:75]
  ds_read_b64_tr_b16 a[18:19], v5 offset:39424
  v_mfma_f32_16x16x32_bf16 v[68:71], a[24:27], a[36:39], v[68:71]
  ds_read_b128 a[56:59], v9 offset:1792
  v_mfma_f32_16x16x32_bf16 v[64:67], a[24:27], a[32:35], v[64:67]
  buffer_load_dwordx4 v2, s[16:19], s45 offen offset:2048 lds
  v_mfma_f32_16x16x32_bf16 v[80:83], a[0:3], a[32:35], v[80:83]
  ds_read_b128 a[60:63], v9 offset:6400
  v_mfma_f32_16x16x32_bf16 v[84:87], a[0:3], a[36:39], v[84:87]
  ds_read_b64_tr_b16 a[20:21], v5 offset:37408
  v_mfma_f32_16x16x32_bf16 v[88:91], a[0:3], a[40:43], v[88:91]
  ds_read_b64_tr_b16 a[22:23], v5 offset:39712
  v_mfma_f32_16x16x32_bf16 v[92:95], a[0:3], a[44:47], v[92:95]
  ds_read_b64_tr_b16 a[24:25], v3 offset:55488
  v_mfma_f32_16x16x32_bf16 v[108:111], a[4:7], a[44:47], v[108:111]
  ds_read_b64_tr_b16 a[26:27], v3 offset:57792
  v_mfma_f32_16x16x32_bf16 v[104:107], a[4:7], a[40:43], v[104:107]
  buffer_load_dwordx4 v2, s[16:19], s45 offen offset:3072 lds
  v_mfma_f32_16x16x32_bf16 v[100:103], a[4:7], a[36:39], v[100:103]
  ds_read_b64_tr_b16 a[0:1], v3 offset:55776
  v_mfma_f32_16x16x32_bf16 v[96:99], a[4:7], a[32:35], v[96:99]
  ds_read_b64_tr_b16 a[2:3], v3 offset:58080
  s_waitcnt lgkmcnt(5)
  v_mfma_f32_16x16x32_bf16 v[112:115], a[8:11], a[32:35], v[112:115]
  ds_read_b128 a[96:99], v7 offset:768
  v_mfma_f32_16x16x32_bf16 v[116:119], a[8:11], a[36:39], v[116:119]
  ds_read_b64_tr_b16 a[4:5], v3 offset:55520
  v_mfma_f32_16x16x32_bf16 v[120:123], a[8:11], a[40:43], v[120:123]
  ds_read_b64_tr_b16 a[6:7], v3 offset:57824
  v_mfma_f32_16x16x32_bf16 v[124:127], a[8:11], a[44:47], v[124:127]
  buffer_load_dwordx4 v2, s[16:19], s45 offen offset:3584 lds
  v_mfma_f32_16x16x32_bf16 v[140:143], a[12:15], a[44:47], v[140:143]
  ds_read_b128 a[100:103], v7 offset:5376
  v_mfma_f32_16x16x32_bf16 v[136:139], a[12:15], a[40:43], v[136:139]
  ds_read_b64_tr_b16 a[8:9], v3 offset:55808
  v_mfma_f32_16x16x32_bf16 v[132:135], a[12:15], a[36:39], v[132:135]
  ds_read_b64_tr_b16 a[10:11], v3 offset:58112
  v_mfma_f32_16x16x32_bf16 v[128:131], a[12:15], a[32:35], v[128:131]
  ds_read_b64_tr_b16 a[12:13], v3 offset:55552
  v_mfma_f32_16x16x32_bf16 v[144:147], a[16:19], a[32:35], v[144:147]
  ds_read_b64_tr_b16 a[14:15], v3 offset:57856
  v_mfma_f32_16x16x32_bf16 v[148:151], a[16:19], a[36:39], v[148:151]
  s_waitcnt lgkmcnt(5)
  v_mfma_f32_16x16x32_bf16 v[156:159], a[20:23], a[36:39], v[156:159]
  v_mfma_f32_16x16x32_bf16 v[152:155], a[20:23], a[32:35], v[152:155]
  ds_read_b64_tr_b16 a[16:17], v3 offset:55840
  v_mfma_f32_16x16x32_bf16 v[16:19], a[24:27], a[48:51], v[16:19]
  ds_read_b64_tr_b16 a[18:19], v3 offset:58144
  v_mfma_f32_16x16x32_bf16 v[20:23], a[24:27], a[52:55], v[20:23]
  v_mfma_f32_16x16x32_bf16 v[24:27], a[24:27], a[56:59], v[24:27]
  s_waitcnt vmcnt(16)
  s_barrier
  v_mfma_f32_16x16x32_bf16 v[28:31], a[24:27], a[60:63], v[28:31]
  s_add_u32 m0, s41, 36992
  s_add_u32 s45, s40, 119808
  v_mfma_f32_16x16x32_bf16 v[44:47], a[0:3], a[60:63], v[44:47]
  buffer_load_dwordx4 v2, s[16:19], s45 offen lds
  v_mfma_f32_16x16x32_bf16 v[40:43], a[0:3], a[56:59], v[40:43]
  ds_read_b64_tr_b16 a[20:21], v3 offset:55584
  v_mfma_f32_16x16x32_bf16 v[36:39], a[0:3], a[52:55], v[36:39]
  ds_read_b64_tr_b16 a[22:23], v3 offset:57888
  v_mfma_f32_16x16x32_bf16 v[32:35], a[0:3], a[48:51], v[32:35]
  ds_read_b64_tr_b16 a[24:25], v3 offset:55872
  v_mfma_f32_16x16x32_bf16 v[48:51], a[4:7], a[48:51], v[48:51]
  ds_read_b64_tr_b16 a[26:27], v3 offset:58176
  v_mfma_f32_16x16x32_bf16 v[52:55], a[4:7], a[52:55], v[52:55]
  buffer_load_dwordx4 v2, s[16:19], s45 offen offset:1024 lds
  v_mfma_f32_16x16x32_bf16 v[56:59], a[4:7], a[56:59], v[56:59]
  ds_read_b128 a[32:35], v7 offset:1216
  v_mfma_f32_16x16x32_bf16 v[60:63], a[4:7], a[60:63], v[60:63]
  ds_read_b128 a[36:39], v7 offset:5824
  s_waitcnt lgkmcnt(5)
  v_mfma_f32_16x16x32_bf16 v[76:79], a[8:11], a[60:63], v[76:79]
  ds_read_b64_tr_b16 a[0:1], v5 offset:55616
  v_mfma_f32_16x16x32_bf16 v[72:75], a[8:11], a[56:59], v[72:75]
  ds_read_b64_tr_b16 a[2:3], v5 offset:57920
  v_mfma_f32_16x16x32_bf16 v[68:71], a[8:11], a[52:55], v[68:71]
  ds_read_b128 a[40:43], v9 offset:1216
  v_mfma_f32_16x16x32_bf16 v[64:67], a[8:11], a[48:51], v[64:67]
  buffer_load_dwordx4 v2, s[16:19], s45 offen offset:2048 lds
  v_mfma_f32_16x16x32_bf16 v[80:83], a[12:15], a[48:51], v[80:83]
  ds_read_b128 a[44:47], v9 offset:5824
  v_mfma_f32_16x16x32_bf16 v[84:87], a[12:15], a[52:55], v[84:87]
  ds_read_b64_tr_b16 a[4:5], v5 offset:55904
  v_mfma_f32_16x16x32_bf16 v[88:91], a[12:15], a[56:59], v[88:91]
  ds_read_b64_tr_b16 a[6:7], v5 offset:58208
  v_mfma_f32_16x16x32_bf16 v[92:95], a[12:15], a[60:63], v[92:95]
  ds_read_b64_tr_b16 a[8:9], v4
  v_mfma_f32_16x16x32_bf16 v[108:111], a[16:19], a[60:63], v[108:111]
  ds_read_b64_tr_b16 a[10:11], v4 offset:2304
  v_mfma_f32_16x16x32_bf16 v[104:107], a[16:19], a[56:59], v[104:107]
  buffer_load_dwordx4 v2, s[16:19], s45 offen offset:3072 lds
  v_mfma_f32_16x16x32_bf16 v[100:103], a[16:19], a[52:55], v[100:103]
  ds_read_b64_tr_b16 a[12:13], v4 offset:288
  v_mfma_f32_16x16x32_bf16 v[96:99], a[16:19], a[48:51], v[96:99]
  ds_read_b64_tr_b16 a[14:15], v4 offset:2592
  s_waitcnt lgkmcnt(5)
  v_mfma_f32_16x16x32_bf16 v[112:115], a[20:23], a[48:51], v[112:115]
  ds_read_b128 a[104:107], v9 offset:768
  v_mfma_f32_16x16x32_bf16 v[116:119], a[20:23], a[52:55], v[116:119]
  ds_read_b64_tr_b16 a[16:17], v4 offset:32
  v_mfma_f32_16x16x32_bf16 v[120:123], a[20:23], a[56:59], v[120:123]
  ds_read_b64_tr_b16 a[18:19], v4 offset:2336
  v_mfma_f32_16x16x32_bf16 v[124:127], a[20:23], a[60:63], v[124:127]
  buffer_load_dwordx4 v2, s[16:19], s45 offen offset:3584 lds
  v_mfma_f32_16x16x32_bf16 v[140:143], a[24:27], a[60:63], v[140:143]
  ds_read_b128 a[108:111], v9 offset:5376
  v_mfma_f32_16x16x32_bf16 v[136:139], a[24:27], a[56:59], v[136:139]
  ds_read_b64_tr_b16 a[20:21], v4 offset:320
  v_mfma_f32_16x16x32_bf16 v[132:135], a[24:27], a[52:55], v[132:135]
  ds_read_b64_tr_b16 a[22:23], v4 offset:2624
  v_mfma_f32_16x16x32_bf16 v[128:131], a[24:27], a[48:51], v[128:131]
  ds_read_b64_tr_b16 a[24:25], v4 offset:64
  v_mfma_f32_16x16x32_bf16 v[144:147], a[0:3], a[48:51], v[144:147]
  ds_read_b64_tr_b16 a[26:27], v4 offset:2368
  v_mfma_f32_16x16x32_bf16 v[148:151], a[0:3], a[52:55], v[148:151]
  s_waitcnt lgkmcnt(5)
  v_mfma_f32_16x16x32_bf16 v[156:159], a[4:7], a[52:55], v[156:159]
  v_mfma_f32_16x16x32_bf16 v[152:155], a[4:7], a[48:51], v[152:155]
  ds_read_b64_tr_b16 a[0:1], v4 offset:352
  v_mfma_f32_16x16x32_bf16 v[16:19], a[8:11], a[32:35], v[16:19]
  ds_read_b64_tr_b16 a[2:3], v4 offset:2656
  v_mfma_f32_16x16x32_bf16 v[20:23], a[8:11], a[36:39], v[20:23]
  v_mfma_f32_16x16x32_bf16 v[24:27], a[8:11], a[40:43], v[24:27]
  s_waitcnt vmcnt(16)
  s_barrier
  v_mfma_f32_16x16x32_bf16 v[28:31], a[8:11], a[44:47], v[28:31]
  s_add_u32 m0, s41, 55488
  s_add_u32 s45, s40, 202752
  v_mfma_f32_16x16x32_bf16 v[44:47], a[12:15], a[44:47], v[44:47]
  buffer_load_dwordx4 v2, s[16:19], s45 offen lds
  v_mfma_f32_16x16x32_bf16 v[40:43], a[12:15], a[40:43], v[40:43]
  ds_read_b64_tr_b16 a[4:5], v4 offset:96
  v_mfma_f32_16x16x32_bf16 v[36:39], a[12:15], a[36:39], v[36:39]
  ds_read_b64_tr_b16 a[6:7], v4 offset:2400
  v_mfma_f32_16x16x32_bf16 v[32:35], a[12:15], a[32:35], v[32:35]
  ds_read_b64_tr_b16 a[8:9], v4 offset:384
  v_mfma_f32_16x16x32_bf16 v[48:51], a[16:19], a[32:35], v[48:51]
  ds_read_b64_tr_b16 a[10:11], v4 offset:2688
  v_mfma_f32_16x16x32_bf16 v[52:55], a[16:19], a[36:39], v[52:55]
  buffer_load_dwordx4 v2, s[16:19], s45 offen offset:1024 lds
  v_mfma_f32_16x16x32_bf16 v[56:59], a[16:19], a[40:43], v[56:59]
  ds_read_b128 a[48:51], v7 offset:640
  v_mfma_f32_16x16x32_bf16 v[60:63], a[16:19], a[44:47], v[60:63]
  ds_read_b128 a[52:55], v7 offset:5248
  s_waitcnt lgkmcnt(5)
  v_mfma_f32_16x16x32_bf16 v[76:79], a[20:23], a[44:47], v[76:79]
  ds_read_b64_tr_b16 a[12:13], v6 offset:128
  v_mfma_f32_16x16x32_bf16 v[72:75], a[20:23], a[40:43], v[72:75]
  ds_read_b64_tr_b16 a[14:15], v6 offset:2432
  v_mfma_f32_16x16x32_bf16 v[68:71], a[20:23], a[36:39], v[68:71]
  ds_read_b128 a[56:59], v9 offset:640
  v_mfma_f32_16x16x32_bf16 v[64:67], a[20:23], a[32:35], v[64:67]
  buffer_load_dwordx4 v2, s[16:19], s45 offen offset:2048 lds
  v_mfma_f32_16x16x32_bf16 v[80:83], a[24:27], a[32:35], v[80:83]
  ds_read_b128 a[60:63], v9 offset:5248
  v_mfma_f32_16x16x32_bf16 v[84:87], a[24:27], a[36:39], v[84:87]
  ds_read_b64_tr_b16 a[16:17], v6 offset:416
  v_mfma_f32_16x16x32_bf16 v[88:91], a[24:27], a[40:43], v[88:91]
  ds_read_b64_tr_b16 a[18:19], v6 offset:2720
  v_mfma_f32_16x16x32_bf16 v[92:95], a[24:27], a[44:47], v[92:95]
  ds_read_b64_tr_b16 a[20:21], v4 offset:18496
  v_mfma_f32_16x16x32_bf16 v[108:111], a[0:3], a[44:47], v[108:111]
  ds_read_b64_tr_b16 a[22:23], v4 offset:20800
  v_mfma_f32_16x16x32_bf16 v[104:107], a[0:3], a[40:43], v[104:107]
  buffer_load_dwordx4 v2, s[16:19], s45 offen offset:3072 lds
  v_mfma_f32_16x16x32_bf16 v[100:103], a[0:3], a[36:39], v[100:103]
  ds_read_b64_tr_b16 a[24:25], v4 offset:18784
  v_mfma_f32_16x16x32_bf16 v[96:99], a[0:3], a[32:35], v[96:99]
  ds_read_b64_tr_b16 a[26:27], v4 offset:21088
  s_waitcnt lgkmcnt(5)
  v_mfma_f32_16x16x32_bf16 v[112:115], a[4:7], a[32:35], v[112:115]
  ds_read_b128 a[112:115], v7 offset:192
  v_mfma_f32_16x16x32_bf16 v[116:119], a[4:7], a[36:39], v[116:119]
  ds_read_b64_tr_b16 a[0:1], v4 offset:18528
  v_mfma_f32_16x16x32_bf16 v[120:123], a[4:7], a[40:43], v[120:123]
  ds_read_b64_tr_b16 a[2:3], v4 offset:20832
  v_mfma_f32_16x16x32_bf16 v[124:127], a[4:7], a[44:47], v[124:127]
  buffer_load_dwordx4 v2, s[16:19], s45 offen offset:3584 lds
  v_mfma_f32_16x16x32_bf16 v[140:143], a[8:11], a[44:47], v[140:143]
  ds_read_b128 a[116:119], v7 offset:4800
  v_mfma_f32_16x16x32_bf16 v[136:139], a[8:11], a[40:43], v[136:139]
  ds_read_b64_tr_b16 a[4:5], v4 offset:18816
  v_mfma_f32_16x16x32_bf16 v[132:135], a[8:11], a[36:39], v[132:135]
  ds_read_b64_tr_b16 a[6:7], v4 offset:21120
  v_mfma_f32_16x16x32_bf16 v[128:131], a[8:11], a[32:35], v[128:131]
  ds_read_b64_tr_b16 a[8:9], v4 offset:18560
  v_mfma_f32_16x16x32_bf16 v[144:147], a[12:15], a[32:35], v[144:147]
  ds_read_b64_tr_b16 a[10:11], v4 offset:20864
  v_mfma_f32_16x16x32_bf16 v[148:151], a[12:15], a[36:39], v[148:151]
  s_waitcnt lgkmcnt(5)
  v_mfma_f32_16x16x32_bf16 v[156:159], a[16:19], a[36:39], v[156:159]
  v_mfma_f32_16x16x32_bf16 v[152:155], a[16:19], a[32:35], v[152:155]
  ds_read_b64_tr_b16 a[12:13], v4 offset:18848
  v_mfma_f32_16x16x32_bf16 v[16:19], a[20:23], a[48:51], v[16:19]
  ds_read_b64_tr_b16 a[14:15], v4 offset:21152
  v_mfma_f32_16x16x32_bf16 v[20:23], a[20:23], a[52:55], v[20:23]
  v_mfma_f32_16x16x32_bf16 v[24:27], a[20:23], a[56:59], v[24:27]
  s_waitcnt vmcnt(16)
  s_barrier
  v_mfma_f32_16x16x32_bf16 v[28:31], a[20:23], a[60:63], v[28:31]
  s_add_u32 m0, s41, 73984
  s_add_u32 s45, s40, 285696
  v_mfma_f32_16x16x32_bf16 v[44:47], a[24:27], a[60:63], v[44:47]
  buffer_load_dwordx4 v2, s[16:19], s45 offen lds
  v_mfma_f32_16x16x32_bf16 v[40:43], a[24:27], a[56:59], v[40:43]
  ds_read_b64_tr_b16 a[16:17], v4 offset:18592
  v_mfma_f32_16x16x32_bf16 v[36:39], a[24:27], a[52:55], v[36:39]
  ds_read_b64_tr_b16 a[18:19], v4 offset:20896
  v_mfma_f32_16x16x32_bf16 v[32:35], a[24:27], a[48:51], v[32:35]
  ds_read_b64_tr_b16 a[20:21], v4 offset:18880
  v_mfma_f32_16x16x32_bf16 v[48:51], a[0:3], a[48:51], v[48:51]
  ds_read_b64_tr_b16 a[22:23], v4 offset:21184
  v_mfma_f32_16x16x32_bf16 v[52:55], a[0:3], a[52:55], v[52:55]
  buffer_load_dwordx4 v2, s[16:19], s45 offen offset:1024 lds
  v_mfma_f32_16x16x32_bf16 v[56:59], a[0:3], a[56:59], v[56:59]
  ds_read_b128 a[32:35], v7 offset:64
  v_mfma_f32_16x16x32_bf16 v[60:63], a[0:3], a[60:63], v[60:63]
  ds_read_b128 a[36:39], v7 offset:4672
  s_waitcnt lgkmcnt(5)
  v_mfma_f32_16x16x32_bf16 v[76:79], a[4:7], a[60:63], v[76:79]
  ds_read_b64_tr_b16 a[24:25], v6 offset:18624
  v_mfma_f32_16x16x32_bf16 v[72:75], a[4:7], a[56:59], v[72:75]
  ds_read_b64_tr_b16 a[26:27], v6 offset:20928
  v_mfma_f32_16x16x32_bf16 v[68:71], a[4:7], a[52:55], v[68:71]
  ds_read_b128 a[40:43], v9 offset:64
  v_mfma_f32_16x16x32_bf16 v[64:67], a[4:7], a[48:51], v[64:67]
  buffer_load_dwordx4 v2, s[16:19], s45 offen offset:2048 lds
  v_mfma_f32_16x16x32_bf16 v[80:83], a[8:11], a[48:51], v[80:83]
  ds_read_b128 a[44:47], v9 offset:4672
  v_mfma_f32_16x16x32_bf16 v[84:87], a[8:11], a[52:55], v[84:87]
  ds_read_b64_tr_b16 a[0:1], v6 offset:18912
  v_mfma_f32_16x16x32_bf16 v[88:91], a[8:11], a[56:59], v[88:91]
  ds_read_b64_tr_b16 a[2:3], v6 offset:21216
  v_mfma_f32_16x16x32_bf16 v[92:95], a[8:11], a[60:63], v[92:95]
  ds_read_b64_tr_b16 a[4:5], v3
  v_mfma_f32_16x16x32_bf16 v[108:111], a[12:15], a[60:63], v[108:111]
  ds_read_b64_tr_b16 a[6:7], v3 offset:2304
  v_mfma_f32_16x16x32_bf16 v[104:107], a[12:15], a[56:59], v[104:107]
  buffer_load_dwordx4 v2, s[16:19], s45 offen offset:3072 lds
  v_mfma_f32_16x16x32_bf16 v[100:103], a[12:15], a[52:55], v[100:103]
  ds_read_b64_tr_b16 a[8:9], v3 offset:288
  v_mfma_f32_16x16x32_bf16 v[96:99], a[12:15], a[48:51], v[96:99]
  ds_read_b64_tr_b16 a[10:11], v3 offset:2592
  s_waitcnt lgkmcnt(5)
  v_mfma_f32_16x16x32_bf16 v[112:115], a[16:19], a[48:51], v[112:115]
  ds_read_b128 a[120:123], v9 offset:192
  v_mfma_f32_16x16x32_bf16 v[116:119], a[16:19], a[52:55], v[116:119]
  ds_read_b64_tr_b16 a[12:13], v3 offset:32
  v_mfma_f32_16x16x32_bf16 v[120:123], a[16:19], a[56:59], v[120:123]
  ds_read_b64_tr_b16 a[14:15], v3 offset:2336
  v_mfma_f32_16x16x32_bf16 v[124:127], a[16:19], a[60:63], v[124:127]
  buffer_load_dwordx4 v2, s[16:19], s45 offen offset:3584 lds
  v_mfma_f32_16x16x32_bf16 v[140:143], a[20:23], a[60:63], v[140:143]
  ds_read_b128 a[124:127], v9 offset:4800
  v_mfma_f32_16x16x32_bf16 v[136:139], a[20:23], a[56:59], v[136:139]
  ds_read_b64_tr_b16 a[16:17], v3 offset:320
  v_mfma_f32_16x16x32_bf16 v[132:135], a[20:23], a[52:55], v[132:135]
  ds_read_b64_tr_b16 a[18:19], v3 offset:2624
  v_mfma_f32_16x16x32_bf16 v[128:131], a[20:23], a[48:51], v[128:131]
  ds_read_b64_tr_b16 a[20:21], v3 offset:64
  v_mfma_f32_16x16x32_bf16 v[144:147], a[24:27], a[48:51], v[144:147]
  ds_read_b64_tr_b16 a[22:23], v3 offset:2368
  v_mfma_f32_16x16x32_bf16 v[148:151], a[24:27], a[52:55], v[148:151]
  s_waitcnt lgkmcnt(5)
  v_mfma_f32_16x16x32_bf16 v[156:159], a[0:3], a[52:55], v[156:159]
  v_mfma_f32_16x16x32_bf16 v[152:155], a[0:3], a[48:51], v[152:155]
  ds_read_b64_tr_b16 a[24:25], v3 offset:352
  v_mfma_f32_16x16x32_bf16 v[16:19], a[4:7], a[32:35], v[16:19]
  ds_read_b64_tr_b16 a[26:27], v3 offset:2656
  v_mfma_f32_16x16x32_bf16 v[20:23], a[4:7], a[36:39], v[20:23]
  v_mfma_f32_16x16x32_bf16 v[24:27], a[4:7], a[40:43], v[24:27]
  s_waitcnt vmcnt(15)
  s_barrier
  v_mfma_f32_16x16x32_bf16 v[28:31], a[4:7], a[44:47], v[28:31]
  s_add_u32 m0, s41, 92480
  s_add_u32 s45, s40, 368640
  v_mfma_f32_16x16x32_bf16 v[44:47], a[8:11], a[44:47], v[44:47]
  buffer_load_dwordx4 v2, s[16:19], s45 offen lds
  v_mfma_f32_16x16x32_bf16 v[40:43], a[8:11], a[40:43], v[40:43]
  ds_read_b64_tr_b16 a[0:1], v3 offset:96
  v_mfma_f32_16x16x32_bf16 v[36:39], a[8:11], a[36:39], v[36:39]
  ds_read_b64_tr_b16 a[2:3], v3 offset:2400
  v_mfma_f32_16x16x32_bf16 v[32:35], a[8:11], a[32:35], v[32:35]
  ds_read_b64_tr_b16 a[4:5], v3 offset:384
  v_mfma_f32_16x16x32_bf16 v[48:51], a[12:15], a[32:35], v[48:51]
  ds_read_b64_tr_b16 a[6:7], v3 offset:2688
  v_mfma_f32_16x16x32_bf16 v[52:55], a[12:15], a[36:39], v[52:55]
  buffer_load_dwordx4 v2, s[16:19], s45 offen offset:1024 lds
  v_mfma_f32_16x16x32_bf16 v[56:59], a[12:15], a[40:43], v[56:59]
  ds_read_b128 a[48:51], v7 offset:2432
  v_mfma_f32_16x16x32_bf16 v[60:63], a[12:15], a[44:47], v[60:63]
  ds_read_b128 a[52:55], v7 offset:7040
  s_waitcnt lgkmcnt(5)
  v_mfma_f32_16x16x32_bf16 v[76:79], a[16:19], a[44:47], v[76:79]
  ds_read_b64_tr_b16 a[8:9], v5 offset:128
  v_mfma_f32_16x16x32_bf16 v[72:75], a[16:19], a[40:43], v[72:75]
  ds_read_b64_tr_b16 a[10:11], v5 offset:2432
  v_mfma_f32_16x16x32_bf16 v[68:71], a[16:19], a[36:39], v[68:71]
  ds_read_b128 a[56:59], v9 offset:2432
  v_mfma_f32_16x16x32_bf16 v[64:67], a[16:19], a[32:35], v[64:67]
  buffer_load_dwordx4 v2, s[16:19], s45 offen offset:2048 lds
  v_mfma_f32_16x16x32_bf16 v[80:83], a[20:23], a[32:35], v[80:83]
  ds_read_b128 a[60:63], v9 offset:7040
  v_mfma_f32_16x16x32_bf16 v[84:87], a[20:23], a[36:39], v[84:87]
  ds_read_b64_tr_b16 a[12:13], v5 offset:416
  v_mfma_f32_16x16x32_bf16 v[88:91], a[20:23], a[40:43], v[88:91]
  ds_read_b64_tr_b16 a[14:15], v5 offset:2720
  v_mfma_f32_16x16x32_bf16 v[92:95], a[20:23], a[44:47], v[92:95]
  ds_read_b64_tr_b16 a[16:17], v3 offset:18496
  v_mfma_f32_16x16x32_bf16 v[108:111], a[24:27], a[44:47], v[108:111]
  ds_read_b64_tr_b16 a[18:19], v3 offset:20800
  v_mfma_f32_16x16x32_bf16 v[104:107], a[24:27], a[40:43], v[104:107]
  buffer_load_dwordx4 v2, s[16:19], s45 offen offset:3072 lds
  v_mfma_f32_16x16x32_bf16 v[100:103], a[24:27], a[36:39], v[100:103]
  ds_read_b64_tr_b16 a[20:21], v3 offset:18784
  v_mfma_f32_16x16x32_bf16 v[96:99], a[24:27], a[32:35], v[96:99]
  ds_read_b64_tr_b16 a[22:23], v3 offset:21088
  s_waitcnt lgkmcnt(5)
  v_mfma_f32_16x16x32_bf16 v[112:115], a[0:3], a[32:35], v[112:115]
  ds_read_b64_tr_b16 a[24:25], v3 offset:18528
  v_mfma_f32_16x16x32_bf16 v[116:119], a[0:3], a[36:39], v[116:119]
  ds_read_b64_tr_b16 a[26:27], v3 offset:20832
  v_mfma_f32_16x16x32_bf16 v[120:123], a[0:3], a[40:43], v[120:123]
  v_mfma_f32_16x16x32_bf16 v[124:127], a[0:3], a[44:47], v[124:127]
  buffer_load_dwordx4 v2, s[16:19], s45 offen offset:3584 lds
  v_mfma_f32_16x16x32_bf16 v[140:143], a[4:7], a[44:47], v[140:143]
  ds_read_b64_tr_b16 a[0:1], v3 offset:18816
  v_mfma_f32_16x16x32_bf16 v[136:139], a[4:7], a[40:43], v[136:139]
  ds_read_b64_tr_b16 a[2:3], v3 offset:21120
  v_mfma_f32_16x16x32_bf16 v[132:135], a[4:7], a[36:39], v[132:135]
  v_mfma_f32_16x16x32_bf16 v[128:131], a[4:7], a[32:35], v[128:131]
  ds_read_b64_tr_b16 a[4:5], v3 offset:18560
  v_mfma_f32_16x16x32_bf16 v[144:147], a[8:11], a[32:35], v[144:147]
  ds_read_b64_tr_b16 a[6:7], v3 offset:20864
  v_mfma_f32_16x16x32_bf16 v[148:151], a[8:11], a[36:39], v[148:151]
  s_waitcnt lgkmcnt(4)
  v_mfma_f32_16x16x32_bf16 v[156:159], a[12:15], a[36:39], v[156:159]
  v_mfma_f32_16x16x32_bf16 v[152:155], a[12:15], a[32:35], v[152:155]
  ds_read_b64_tr_b16 a[8:9], v3 offset:18848
  v_mfma_f32_16x16x32_bf16 v[16:19], a[16:19], a[48:51], v[16:19]
  ds_read_b64_tr_b16 a[10:11], v3 offset:21152
  v_mfma_f32_16x16x32_bf16 v[20:23], a[16:19], a[52:55], v[20:23]
  v_mfma_f32_16x16x32_bf16 v[24:27], a[16:19], a[56:59], v[24:27]
  s_waitcnt vmcnt(15)
  s_barrier
  v_mfma_f32_16x16x32_bf16 v[28:31], a[16:19], a[60:63], v[28:31]
  s_add_u32 m0, s41, 0
  s_add_u32 s45, s40, 55296
  v_mfma_f32_16x16x32_bf16 v[44:47], a[20:23], a[60:63], v[44:47]
  buffer_load_dwordx4 v2, s[16:19], s45 offen lds
  v_mfma_f32_16x16x32_bf16 v[40:43], a[20:23], a[56:59], v[40:43]
  ds_read_b64_tr_b16 a[12:13], v3 offset:18592
  v_mfma_f32_16x16x32_bf16 v[36:39], a[20:23], a[52:55], v[36:39]
  ds_read_b64_tr_b16 a[14:15], v3 offset:20896
  v_mfma_f32_16x16x32_bf16 v[32:35], a[20:23], a[48:51], v[32:35]
  ds_read_b64_tr_b16 a[16:17], v3 offset:18880
  v_mfma_f32_16x16x32_bf16 v[48:51], a[24:27], a[48:51], v[48:51]
  ds_read_b64_tr_b16 a[18:19], v3 offset:21184
  v_mfma_f32_16x16x32_bf16 v[52:55], a[24:27], a[52:55], v[52:55]
  buffer_load_dwordx4 v2, s[16:19], s45 offen offset:1024 lds
  v_mfma_f32_16x16x32_bf16 v[56:59], a[24:27], a[56:59], v[56:59]
  ds_read_b128 a[32:35], v7 offset:1856
  v_mfma_f32_16x16x32_bf16 v[60:63], a[24:27], a[60:63], v[60:63]
  ds_read_b128 a[36:39], v7 offset:6464
  s_waitcnt lgkmcnt(5)
  v_mfma_f32_16x16x32_bf16 v[76:79], a[0:3], a[60:63], v[76:79]
  ds_read_b64_tr_b16 a[20:21], v5 offset:18624
  v_mfma_f32_16x16x32_bf16 v[72:75], a[0:3], a[56:59], v[72:75]
  ds_read_b64_tr_b16 a[22:23], v5 offset:20928
  v_mfma_f32_16x16x32_bf16 v[68:71], a[0:3], a[52:55], v[68:71]
  ds_read_b128 a[40:43], v9 offset:1856
  v_mfma_f32_16x16x32_bf16 v[64:67], a[0:3], a[48:51], v[64:67]
  buffer_load_dwordx4 v2, s[16:19], s45 offen offset:2048 lds
  v_mfma_f32_16x16x32_bf16 v[80:83], a[4:7], a[48:51], v[80:83]
  ds_read_b128 a[44:47], v9 offset:6464
  v_mfma_f32_16x16x32_bf16 v[84:87], a[4:7], a[52:55], v[84:87]
  ds_read_b64_tr_b16 a[24:25], v5 offset:18912
  v_mfma_f32_16x16x32_bf16 v[88:91], a[4:7], a[56:59], v[88:91]
  ds_read_b64_tr_b16 a[26:27], v5 offset:21216
  v_mfma_f32_16x16x32_bf16 v[92:95], a[4:7], a[60:63], v[92:95]
  ds_read_b64_tr_b16 a[0:1], v3 offset:36992
  v_mfma_f32_16x16x32_bf16 v[108:111], a[8:11], a[60:63], v[108:111]
  ds_read_b64_tr_b16 a[2:3], v3 offset:39296
  v_mfma_f32_16x16x32_bf16 v[104:107], a[8:11], a[56:59], v[104:107]
  buffer_load_dwordx4 v2, s[16:19], s45 offen offset:3072 lds
  v_mfma_f32_16x16x32_bf16 v[100:103], a[8:11], a[52:55], v[100:103]
  ds_read_b64_tr_b16 a[4:5], v3 offset:37280
  v_mfma_f32_16x16x32_bf16 v[96:99], a[8:11], a[48:51], v[96:99]
  ds_read_b64_tr_b16 a[6:7], v3 offset:39584
  s_waitcnt lgkmcnt(5)
  v_mfma_f32_16x16x32_bf16 v[112:115], a[12:15], a[48:51], v[112:115]
  ds_read_b64_tr_b16 a[8:9], v3 offset:37024
  v_mfma_f32_16x16x32_bf16 v[116:119], a[12:15], a[52:55], v[116:119]
  ds_read_b64_tr_b16 a[10:11], v3 offset:39328
  v_mfma_f32_16x16x32_bf16 v[120:123], a[12:15], a[56:59], v[120:123]
  v_mfma_f32_16x16x32_bf16 v[124:127], a[12:15], a[60:63], v[124:127]
  buffer_load_dwordx4 v2, s[16:19], s45 offen offset:3584 lds
  v_mfma_f32_16x16x32_bf16 v[140:143], a[16:19], a[60:63], v[140:143]
  ds_read_b64_tr_b16 a[12:13], v3 offset:37312
  v_mfma_f32_16x16x32_bf16 v[136:139], a[16:19], a[56:59], v[136:139]
  ds_read_b64_tr_b16 a[14:15], v3 offset:39616
  v_mfma_f32_16x16x32_bf16 v[132:135], a[16:19], a[52:55], v[132:135]
  v_mfma_f32_16x16x32_bf16 v[128:131], a[16:19], a[48:51], v[128:131]
  ds_read_b64_tr_b16 a[16:17], v3 offset:37056
  v_mfma_f32_16x16x32_bf16 v[144:147], a[20:23], a[48:51], v[144:147]
  ds_read_b64_tr_b16 a[18:19], v3 offset:39360
  v_mfma_f32_16x16x32_bf16 v[148:151], a[20:23], a[52:55], v[148:151]
  s_waitcnt lgkmcnt(4)
  v_mfma_f32_16x16x32_bf16 v[156:159], a[24:27], a[52:55], v[156:159]
  v_mfma_f32_16x16x32_bf16 v[152:155], a[24:27], a[48:51], v[152:155]
  ds_read_b64_tr_b16 a[20:21], v3 offset:37344
  v_mfma_f32_16x16x32_bf16 v[16:19], a[0:3], a[32:35], v[16:19]
  ds_read_b64_tr_b16 a[22:23], v3 offset:39648
  v_mfma_f32_16x16x32_bf16 v[20:23], a[0:3], a[36:39], v[20:23]
  v_mfma_f32_16x16x32_bf16 v[24:27], a[0:3], a[40:43], v[24:27]
  s_waitcnt vmcnt(15)
  s_barrier
  v_mfma_f32_16x16x32_bf16 v[28:31], a[0:3], a[44:47], v[28:31]
  s_add_u32 m0, s41, 18496
  s_add_u32 s45, s40, 138240
  v_mfma_f32_16x16x32_bf16 v[44:47], a[4:7], a[44:47], v[44:47]
  buffer_load_dwordx4 v2, s[16:19], s45 offen lds
  v_mfma_f32_16x16x32_bf16 v[40:43], a[4:7], a[40:43], v[40:43]
  ds_read_b64_tr_b16 a[24:25], v3 offset:37088
  v_mfma_f32_16x16x32_bf16 v[36:39], a[4:7], a[36:39], v[36:39]
  ds_read_b64_tr_b16 a[26:27], v3 offset:39392
  v_mfma_f32_16x16x32_bf16 v[32:35], a[4:7], a[32:35], v[32:35]
  ds_read_b64_tr_b16 a[0:1], v3 offset:37376
  v_mfma_f32_16x16x32_bf16 v[48:51], a[8:11], a[32:35], v[48:51]
  ds_read_b64_tr_b16 a[2:3], v3 offset:39680
  v_mfma_f32_16x16x32_bf16 v[52:55], a[8:11], a[36:39], v[52:55]
  buffer_load_dwordx4 v2, s[16:19], s45 offen offset:1024 lds
  v_mfma_f32_16x16x32_bf16 v[56:59], a[8:11], a[40:43], v[56:59]
  ds_read_b128 a[48:51], v7 offset:1280
  v_mfma_f32_16x16x32_bf16 v[60:63], a[8:11], a[44:47], v[60:63]
  ds_read_b128 a[52:55], v7 offset:5888
  s_waitcnt lgkmcnt(5)
  v_mfma_f32_16x16x32_bf16 v[76:79], a[12:15], a[44:47], v[76:79]
  ds_read_b64_tr_b16 a[4:5], v5 offset:37120
  v_mfma_f32_16x16x32_bf16 v[72:75], a[12:15], a[40:43], v[72:75]
  ds_read_b64_tr_b16 a[6:7], v5 offset:39424
  v_mfma_f32_16x16x32_bf16 v[68:71], a[12:15], a[36:39], v[68:71]
  ds_read_b128 a[56:59], v9 offset:1280
  v_mfma_f32_16x16x32_bf16 v[64:67], a[12:15], a[32:35], v[64:67]
  buffer_load_dwordx4 v2, s[16:19], s45 offen offset:2048 lds
  v_mfma_f32_16x16x32_bf16 v[80:83], a[16:19], a[32:35], v[80:83]
  ds_read_b128 a[60:63], v9 offset:5888
  v_mfma_f32_16x16x32_bf16 v[84:87], a[16:19], a[36:39], v[84:87]
  ds_read_b64_tr_b16 a[8:9], v5 offset:37408
  v_mfma_f32_16x16x32_bf16 v[88:91], a[16:19], a[40:43], v[88:91]
  ds_read_b64_tr_b16 a[10:11], v5 offset:39712
  v_mfma_f32_16x16x32_bf16 v[92:95], a[16:19], a[44:47], v[92:95]
  ds_read_b64_tr_b16 a[12:13], v3 offset:55488
  v_mfma_f32_16x16x32_bf16 v[108:111], a[20:23], a[44:47], v[108:111]
  ds_read_b64_tr_b16 a[14:15], v3 offset:57792
  v_mfma_f32_16x16x32_bf16 v[104:107], a[20:23], a[40:43], v[104:107]
  buffer_load_dwordx4 v2, s[16:19], s45 offen offset:3072 lds
  v_mfma_f32_16x16x32_bf16 v[100:103], a[20:23], a[36:39], v[100:103]
  ds_read_b64_tr_b16 a[16:17], v3 offset:55776
  v_mfma_f32_16x16x32_bf16 v[96:99], a[20:23], a[32:35], v[96:99]
  ds_read_b64_tr_b16 a[18:19], v3 offset:58080
  s_waitcnt lgkmcnt(5)
  v_mfma_f32_16x16x32_bf16 v[112:115], a[24:27], a[32:35], v[112:115]
  ds_read_b64_tr_b16 a[20:21], v3 offset:55520
  v_mfma_f32_16x16x32_bf16 v[116:119], a[24:27], a[36:39], v[116:119]
  ds_read_b64_tr_b16 a[22:23], v3 offset:57824
  v_mfma_f32_16x16x32_bf16 v[120:123], a[24:27], a[40:43], v[120:123]
  v_mfma_f32_16x16x32_bf16 v[124:127], a[24:27], a[44:47], v[124:127]
  buffer_load_dwordx4 v2, s[16:19], s45 offen offset:3584 lds
  v_mfma_f32_16x16x32_bf16 v[140:143], a[0:3], a[44:47], v[140:143]
  ds_read_b64_tr_b16 a[24:25], v3 offset:55808
  v_mfma_f32_16x16x32_bf16 v[136:139], a[0:3], a[40:43], v[136:139]
  ds_read_b64_tr_b16 a[26:27], v3 offset:58112
  v_mfma_f32_16x16x32_bf16 v[132:135], a[0:3], a[36:39], v[132:135]
  v_mfma_f32_16x16x32_bf16 v[128:131], a[0:3], a[32:35], v[128:131]
  ds_read_b64_tr_b16 a[0:1], v3 offset:55552
  v_mfma_f32_16x16x32_bf16 v[144:147], a[4:7], a[32:35], v[144:147]
  ds_read_b64_tr_b16 a[2:3], v3 offset:57856
  v_mfma_f32_16x16x32_bf16 v[148:151], a[4:7], a[36:39], v[148:151]
  s_waitcnt lgkmcnt(4)
  v_mfma_f32_16x16x32_bf16 v[156:159], a[8:11], a[36:39], v[156:159]
  v_mfma_f32_16x16x32_bf16 v[152:155], a[8:11], a[32:35], v[152:155]
  ds_read_b64_tr_b16 a[4:5], v3 offset:55840
  v_mfma_f32_16x16x32_bf16 v[16:19], a[12:15], a[48:51], v[16:19]
  ds_read_b64_tr_b16 a[6:7], v3 offset:58144
  v_mfma_f32_16x16x32_bf16 v[20:23], a[12:15], a[52:55], v[20:23]
  v_mfma_f32_16x16x32_bf16 v[24:27], a[12:15], a[56:59], v[24:27]
  s_waitcnt vmcnt(15)
  s_barrier
  v_mfma_f32_16x16x32_bf16 v[28:31], a[12:15], a[60:63], v[28:31]
  s_add_u32 m0, s41, 36992
  s_add_u32 s45, s40, 221184
  v_mfma_f32_16x16x32_bf16 v[44:47], a[16:19], a[60:63], v[44:47]
  buffer_load_dwordx4 v2, s[16:19], s45 offen lds
  v_mfma_f32_16x16x32_bf16 v[40:43], a[16:19], a[56:59], v[40:43]
  ds_read_b64_tr_b16 a[8:9], v3 offset:55584
  v_mfma_f32_16x16x32_bf16 v[36:39], a[16:19], a[52:55], v[36:39]
  ds_read_b64_tr_b16 a[10:11], v3 offset:57888
  v_mfma_f32_16x16x32_bf16 v[32:35], a[16:19], a[48:51], v[32:35]
  ds_read_b64_tr_b16 a[12:13], v3 offset:55872
  v_mfma_f32_16x16x32_bf16 v[48:51], a[20:23], a[48:51], v[48:51]
  ds_read_b64_tr_b16 a[14:15], v3 offset:58176
  v_mfma_f32_16x16x32_bf16 v[52:55], a[20:23], a[52:55], v[52:55]
  buffer_load_dwordx4 v2, s[16:19], s45 offen offset:1024 lds
  v_mfma_f32_16x16x32_bf16 v[56:59], a[20:23], a[56:59], v[56:59]
  ds_read_b128 a[32:35], v7 offset:704
  v_mfma_f32_16x16x32_bf16 v[60:63], a[20:23], a[60:63], v[60:63]
  ds_read_b128 a[36:39], v7 offset:5312
  s_waitcnt lgkmcnt(5)
  v_mfma_f32_16x16x32_bf16 v[76:79], a[24:27], a[60:63], v[76:79]
  ds_read_b64_tr_b16 a[16:17], v5 offset:55616
  v_mfma_f32_16x16x32_bf16 v[72:75], a[24:27], a[56:59], v[72:75]
  ds_read_b64_tr_b16 a[18:19], v5 offset:57920
  v_mfma_f32_16x16x32_bf16 v[68:71], a[24:27], a[52:55], v[68:71]
  ds_read_b128 a[40:43], v9 offset:704
  v_mfma_f32_16x16x32_bf16 v[64:67], a[24:27], a[48:51], v[64:67]
  buffer_load_dwordx4 v2, s[16:19], s45 offen offset:2048 lds
  v_mfma_f32_16x16x32_bf16 v[80:83], a[0:3], a[48:51], v[80:83]
  ds_read_b128 a[44:47], v9 offset:5312
  v_mfma_f32_16x16x32_bf16 v[84:87], a[0:3], a[52:55], v[84:87]
  ds_read_b64_tr_b16 a[20:21], v5 offset:55904
  v_mfma_f32_16x16x32_bf16 v[88:91], a[0:3], a[56:59], v[88:91]
  ds_read_b64_tr_b16 a[22:23], v5 offset:58208
  v_mfma_f32_16x16x32_bf16 v[92:95], a[0:3], a[60:63], v[92:95]
  ds_read_b64_tr_b16 a[24:25], v4
  v_mfma_f32_16x16x32_bf16 v[108:111], a[4:7], a[60:63], v[108:111]
  ds_read_b64_tr_b16 a[26:27], v4 offset:2304
  v_mfma_f32_16x16x32_bf16 v[104:107], a[4:7], a[56:59], v[104:107]
  buffer_load_dwordx4 v2, s[16:19], s45 offen offset:3072 lds
  v_mfma_f32_16x16x32_bf16 v[100:103], a[4:7], a[52:55], v[100:103]
  ds_read_b64_tr_b16 a[0:1], v4 offset:288
  v_mfma_f32_16x16x32_bf16 v[96:99], a[4:7], a[48:51], v[96:99]
  ds_read_b64_tr_b16 a[2:3], v4 offset:2592
  s_waitcnt lgkmcnt(5)
  v_mfma_f32_16x16x32_bf16 v[112:115], a[8:11], a[48:51], v[112:115]
  ds_read_b64_tr_b16 a[4:5], v4 offset:32
  v_mfma_f32_16x16x32_bf16 v[116:119], a[8:11], a[52:55], v[116:119]
  ds_read_b64_tr_b16 a[6:7], v4 offset:2336
  v_mfma_f32_16x16x32_bf16 v[120:123], a[8:11], a[56:59], v[120:123]
  v_mfma_f32_16x16x32_bf16 v[124:127], a[8:11], a[60:63], v[124:127]
  buffer_load_dwordx4 v2, s[16:19], s45 offen offset:3584 lds
  v_mfma_f32_16x16x32_bf16 v[140:143], a[12:15], a[60:63], v[140:143]
  ds_read_b64_tr_b16 a[8:9], v4 offset:320
  v_mfma_f32_16x16x32_bf16 v[136:139], a[12:15], a[56:59], v[136:139]
  ds_read_b64_tr_b16 a[10:11], v4 offset:2624
  v_mfma_f32_16x16x32_bf16 v[132:135], a[12:15], a[52:55], v[132:135]
  v_mfma_f32_16x16x32_bf16 v[128:131], a[12:15], a[48:51], v[128:131]
  ds_read_b64_tr_b16 a[12:13], v4 offset:64
  v_mfma_f32_16x16x32_bf16 v[144:147], a[16:19], a[48:51], v[144:147]
  ds_read_b64_tr_b16 a[14:15], v4 offset:2368
  v_mfma_f32_16x16x32_bf16 v[148:151], a[16:19], a[52:55], v[148:151]
  s_waitcnt lgkmcnt(4)
  v_mfma_f32_16x16x32_bf16 v[156:159], a[20:23], a[52:55], v[156:159]
  v_mfma_f32_16x16x32_bf16 v[152:155], a[20:23], a[48:51], v[152:155]
  ds_read_b64_tr_b16 a[16:17], v4 offset:352
  v_mfma_f32_16x16x32_bf16 v[16:19], a[24:27], a[32:35], v[16:19]
  ds_read_b64_tr_b16 a[18:19], v4 offset:2656
  v_mfma_f32_16x16x32_bf16 v[20:23], a[24:27], a[36:39], v[20:23]
  v_mfma_f32_16x16x32_bf16 v[24:27], a[24:27], a[40:43], v[24:27]
  s_waitcnt vmcnt(15)
  s_barrier
  v_mfma_f32_16x16x32_bf16 v[28:31], a[24:27], a[44:47], v[28:31]
  s_add_u32 m0, s41, 55488
  s_add_u32 s45, s40, 304128
  v_mfma_f32_16x16x32_bf16 v[44:47], a[0:3], a[44:47], v[44:47]
  buffer_load_dwordx4 v2, s[16:19], s45 offen lds
  v_mfma_f32_16x16x32_bf16 v[40:43], a[0:3], a[40:43], v[40:43]
  ds_read_b64_tr_b16 a[20:21], v4 offset:96
  v_mfma_f32_16x16x32_bf16 v[36:39], a[0:3], a[36:39], v[36:39]
  ds_read_b64_tr_b16 a[22:23], v4 offset:2400
  v_mfma_f32_16x16x32_bf16 v[32:35], a[0:3], a[32:35], v[32:35]
  ds_read_b64_tr_b16 a[24:25], v4 offset:384
  v_mfma_f32_16x16x32_bf16 v[48:51], a[4:7], a[32:35], v[48:51]
  ds_read_b64_tr_b16 a[26:27], v4 offset:2688
  v_mfma_f32_16x16x32_bf16 v[52:55], a[4:7], a[36:39], v[52:55]
  buffer_load_dwordx4 v2, s[16:19], s45 offen offset:1024 lds
  v_mfma_f32_16x16x32_bf16 v[56:59], a[4:7], a[40:43], v[56:59]
  ds_read_b128 a[48:51], v7 offset:128
  v_mfma_f32_16x16x32_bf16 v[60:63], a[4:7], a[44:47], v[60:63]
  ds_read_b128 a[52:55], v7 offset:4736
  s_waitcnt lgkmcnt(5)
  v_mfma_f32_16x16x32_bf16 v[76:79], a[8:11], a[44:47], v[76:79]
  ds_read_b64_tr_b16 a[0:1], v6 offset:128
  v_mfma_f32_16x16x32_bf16 v[72:75], a[8:11], a[40:43], v[72:75]
  ds_read_b64_tr_b16 a[2:3], v6 offset:2432
  v_mfma_f32_16x16x32_bf16 v[68:71], a[8:11], a[36:39], v[68:71]
  ds_read_b128 a[56:59], v9 offset:128
  v_mfma_f32_16x16x32_bf16 v[64:67], a[8:11], a[32:35], v[64:67]
  buffer_load_dwordx4 v2, s[16:19], s45 offen offset:2048 lds
  v_mfma_f32_16x16x32_bf16 v[80:83], a[12:15], a[32:35], v[80:83]
  ds_read_b128 a[60:63], v9 offset:4736
  v_mfma_f32_16x16x32_bf16 v[84:87], a[12:15], a[36:39], v[84:87]
  ds_read_b64_tr_b16 a[4:5], v6 offset:416
  v_mfma_f32_16x16x32_bf16 v[88:91], a[12:15], a[40:43], v[88:91]
  ds_read_b64_tr_b16 a[6:7], v6 offset:2720
  v_mfma_f32_16x16x32_bf16 v[92:95], a[12:15], a[44:47], v[92:95]
  ds_read_b64_tr_b16 a[8:9], v4 offset:18496
  v_mfma_f32_16x16x32_bf16 v[108:111], a[16:19], a[44:47], v[108:111]
  ds_read_b64_tr_b16 a[10:11], v4 offset:20800
  v_mfma_f32_16x16x32_bf16 v[104:107], a[16:19], a[40:43], v[104:107]
  buffer_load_dwordx4 v2, s[16:19], s45 offen offset:3072 lds
  v_mfma_f32_16x16x32_bf16 v[100:103], a[16:19], a[36:39], v[100:103]
  ds_read_b64_tr_b16 a[12:13], v4 offset:18784
  v_mfma_f32_16x16x32_bf16 v[96:99], a[16:19], a[32:35], v[96:99]
  ds_read_b64_tr_b16 a[14:15], v4 offset:21088
  s_waitcnt lgkmcnt(5)
  v_mfma_f32_16x16x32_bf16 v[112:115], a[20:23], a[32:35], v[112:115]
  ds_read_b64_tr_b16 a[16:17], v4 offset:18528
  v_mfma_f32_16x16x32_bf16 v[116:119], a[20:23], a[36:39], v[116:119]
  ds_read_b64_tr_b16 a[18:19], v4 offset:20832
  v_mfma_f32_16x16x32_bf16 v[120:123], a[20:23], a[40:43], v[120:123]
  v_mfma_f32_16x16x32_bf16 v[124:127], a[20:23], a[44:47], v[124:127]
  buffer_load_dwordx4 v2, s[16:19], s45 offen offset:3584 lds
  v_mfma_f32_16x16x32_bf16 v[140:143], a[24:27], a[44:47], v[140:143]
  ds_read_b64_tr_b16 a[20:21], v4 offset:18816
  v_mfma_f32_16x16x32_bf16 v[136:139], a[24:27], a[40:43], v[136:139]
  ds_read_b64_tr_b16 a[22:23], v4 offset:21120
  v_mfma_f32_16x16x32_bf16 v[132:135], a[24:27], a[36:39], v[132:135]
  v_mfma_f32_16x16x32_bf16 v[128:131], a[24:27], a[32:35], v[128:131]
  ds_read_b64_tr_b16 a[24:25], v4 offset:18560
  v_mfma_f32_16x16x32_bf16 v[144:147], a[0:3], a[32:35], v[144:147]
  ds_read_b64_tr_b16 a[26:27], v4 offset:20864
  v_mfma_f32_16x16x32_bf16 v[148:151], a[0:3], a[36:39], v[148:151]
  s_waitcnt lgkmcnt(4)
  v_mfma_f32_16x16x32_bf16 v[156:159], a[4:7], a[36:39], v[156:159]
  v_mfma_f32_16x16x32_bf16 v[152:155], a[4:7], a[32:35], v[152:155]
  ds_read_b64_tr_b16 a[0:1], v4 offset:18848
  v_mfma_f32_16x16x32_bf16 v[16:19], a[8:11], a[48:51], v[16:19]
  ds_read_b64_tr_b16 a[2:3], v4 offset:21152
  v_mfma_f32_16x16x32_bf16 v[20:23], a[8:11], a[52:55], v[20:23]
  v_mfma_f32_16x16x32_bf16 v[24:27], a[8:11], a[56:59], v[24:27]
  s_waitcnt vmcnt(15)
  s_barrier
  v_mfma_f32_16x16x32_bf16 v[28:31], a[8:11], a[60:63], v[28:31]
  s_add_u32 m0, s41, 73984
  s_add_u32 s45, s40, 387072
  v_mfma_f32_16x16x32_bf16 v[44:47], a[12:15], a[60:63], v[44:47]
  buffer_load_dwordx4 v2, s[16:19], s45 offen lds
  v_mfma_f32_16x16x32_bf16 v[40:43], a[12:15], a[56:59], v[40:43]
  ds_read_b64_tr_b16 a[4:5], v4 offset:18592
  v_mfma_f32_16x16x32_bf16 v[36:39], a[12:15], a[52:55], v[36:39]
  ds_read_b64_tr_b16 a[6:7], v4 offset:20896
  v_mfma_f32_16x16x32_bf16 v[32:35], a[12:15], a[48:51], v[32:35]
  ds_read_b64_tr_b16 a[8:9], v4 offset:18880
  v_mfma_f32_16x16x32_bf16 v[48:51], a[16:19], a[48:51], v[48:51]
  ds_read_b64_tr_b16 a[10:11], v4 offset:21184
  v_mfma_f32_16x16x32_bf16 v[52:55], a[16:19], a[52:55], v[52:55]
  buffer_load_dwordx4 v2, s[16:19], s45 offen offset:1024 lds
  v_mfma_f32_16x16x32_bf16 v[56:59], a[16:19], a[56:59], v[56:59]
  ds_read_b128 a[32:35], v7 offset:2496
  v_mfma_f32_16x16x32_bf16 v[60:63], a[16:19], a[60:63], v[60:63]
  ds_read_b128 a[36:39], v7 offset:7104
  s_waitcnt lgkmcnt(5)
  v_mfma_f32_16x16x32_bf16 v[76:79], a[20:23], a[60:63], v[76:79]
  ds_read_b64_tr_b16 a[12:13], v6 offset:18624
  v_mfma_f32_16x16x32_bf16 v[72:75], a[20:23], a[56:59], v[72:75]
  ds_read_b64_tr_b16 a[14:15], v6 offset:20928
  v_mfma_f32_16x16x32_bf16 v[68:71], a[20:23], a[52:55], v[68:71]
  ds_read_b128 a[40:43], v9 offset:2496
  v_mfma_f32_16x16x32_bf16 v[64:67], a[20:23], a[48:51], v[64:67]
  buffer_load_dwordx4 v2, s[16:19], s45 offen offset:2048 lds
  v_mfma_f32_16x16x32_bf16 v[80:83], a[24:27], a[48:51], v[80:83]
  ds_read_b128 a[44:47], v9 offset:7104
  v_mfma_f32_16x16x32_bf16 v[84:87], a[24:27], a[52:55], v[84:87]
  ds_read_b64_tr_b16 a[16:17], v6 offset:18912
  v_mfma_f32_16x16x32_bf16 v[88:91], a[24:27], a[56:59], v[88:91]
  ds_read_b64_tr_b16 a[18:19], v6 offset:21216
  v_mfma_f32_16x16x32_bf16 v[92:95], a[24:27], a[60:63], v[92:95]
  ds_read_b64_tr_b16 a[20:21], v3
  v_mfma_f32_16x16x32_bf16 v[108:111], a[0:3], a[60:63], v[108:111]
  ds_read_b64_tr_b16 a[22:23], v3 offset:2304
  v_mfma_f32_16x16x32_bf16 v[104:107], a[0:3], a[56:59], v[104:107]
  buffer_load_dwordx4 v2, s[16:19], s45 offen offset:3072 lds
  v_mfma_f32_16x16x32_bf16 v[100:103], a[0:3], a[52:55], v[100:103]
  ds_read_b64_tr_b16 a[24:25], v3 offset:288
  v_mfma_f32_16x16x32_bf16 v[96:99], a[0:3], a[48:51], v[96:99]
  ds_read_b64_tr_b16 a[26:27], v3 offset:2592
  s_waitcnt lgkmcnt(5)
  v_mfma_f32_16x16x32_bf16 v[112:115], a[4:7], a[48:51], v[112:115]
  ds_read_b64_tr_b16 a[0:1], v3 offset:32
  v_mfma_f32_16x16x32_bf16 v[116:119], a[4:7], a[52:55], v[116:119]
  ds_read_b64_tr_b16 a[2:3], v3 offset:2336
  v_mfma_f32_16x16x32_bf16 v[120:123], a[4:7], a[56:59], v[120:123]
  v_mfma_f32_16x16x32_bf16 v[124:127], a[4:7], a[60:63], v[124:127]
  buffer_load_dwordx4 v2, s[16:19], s45 offen offset:3584 lds
  v_mfma_f32_16x16x32_bf16 v[140:143], a[8:11], a[60:63], v[140:143]
  ds_read_b64_tr_b16 a[4:5], v3 offset:320
  v_mfma_f32_16x16x32_bf16 v[136:139], a[8:11], a[56:59], v[136:139]
  ds_read_b64_tr_b16 a[6:7], v3 offset:2624
  v_mfma_f32_16x16x32_bf16 v[132:135], a[8:11], a[52:55], v[132:135]
  v_mfma_f32_16x16x32_bf16 v[128:131], a[8:11], a[48:51], v[128:131]
  ds_read_b64_tr_b16 a[8:9], v3 offset:64
  v_mfma_f32_16x16x32_bf16 v[144:147], a[12:15], a[48:51], v[144:147]
  ds_read_b64_tr_b16 a[10:11], v3 offset:2368
  v_mfma_f32_16x16x32_bf16 v[148:151], a[12:15], a[52:55], v[148:151]
  s_waitcnt lgkmcnt(4)
  v_mfma_f32_16x16x32_bf16 v[156:159], a[16:19], a[52:55], v[156:159]
  v_mfma_f32_16x16x32_bf16 v[152:155], a[16:19], a[48:51], v[152:155]
  ds_read_b64_tr_b16 a[12:13], v3 offset:352
  v_mfma_f32_16x16x32_bf16 v[16:19], a[20:23], a[32:35], v[16:19]
  ds_read_b64_tr_b16 a[14:15], v3 offset:2656
  v_mfma_f32_16x16x32_bf16 v[20:23], a[20:23], a[36:39], v[20:23]
  v_mfma_f32_16x16x32_bf16 v[24:27], a[20:23], a[40:43], v[24:27]
  s_waitcnt vmcnt(15)
  s_barrier
  v_mfma_f32_16x16x32_bf16 v[28:31], a[20:23], a[44:47], v[28:31]
  s_add_u32 m0, s50, 0
  s_add_u32 s45, s49, 0
  v_mfma_f32_16x16x32_bf16 v[44:47], a[24:27], a[44:47], v[44:47]
  ds_read_b64_tr_b16 a[16:17], v3 offset:96
  v_mfma_f32_16x16x32_bf16 v[40:43], a[24:27], a[40:43], v[40:43]
  buffer_load_dwordx4 v2, s[24:27], s45 offen lds
  v_mfma_f32_16x16x32_bf16 v[36:39], a[24:27], a[36:39], v[36:39]
  ds_read_b64_tr_b16 a[18:19], v3 offset:2400
  v_mfma_f32_16x16x32_bf16 v[32:35], a[24:27], a[32:35], v[32:35]
  buffer_load_dwordx4 v2, s[24:27], s45 offen offset:1024 lds
  v_mfma_f32_16x16x32_bf16 v[48:51], a[0:3], a[32:35], v[48:51]
  ds_read_b64_tr_b16 a[20:21], v3 offset:384
  v_mfma_f32_16x16x32_bf16 v[52:55], a[0:3], a[36:39], v[52:55]
  buffer_load_dwordx4 v2, s[24:27], s45 offen offset:2048 lds
  v_mfma_f32_16x16x32_bf16 v[56:59], a[0:3], a[40:43], v[56:59]
  ds_read_b64_tr_b16 a[22:23], v3 offset:2688
  v_mfma_f32_16x16x32_bf16 v[60:63], a[0:3], a[44:47], v[60:63]
  buffer_load_dwordx4 v2, s[24:27], s45 offen offset:3072 lds
  s_waitcnt lgkmcnt(3)
  v_mfma_f32_16x16x32_bf16 v[76:79], a[4:7], a[44:47], v[76:79]
  ds_read_b64_tr_b16 a[24:25], v5 offset:128
  v_mfma_f32_16x16x32_bf16 v[72:75], a[4:7], a[40:43], v[72:75]
  s_add_u32 m0, s50, 4096
  s_add_u32 s45, s49, 4096
  v_mfma_f32_16x16x32_bf16 v[68:71], a[4:7], a[36:39], v[68:71]
  ds_read_b64_tr_b16 a[26:27], v5 offset:2432
  v_mfma_f32_16x16x32_bf16 v[64:67], a[4:7], a[32:35], v[64:67]
  buffer_load_dwordx4 v2, s[24:27], s45 offen lds
  v_mfma_f32_16x16x32_bf16 v[80:83], a[8:11], a[32:35], v[80:83]
  ds_read_b64_tr_b16 a[0:1], v5 offset:416
  v_mfma_f32_16x16x32_bf16 v[84:87], a[8:11], a[36:39], v[84:87]
  buffer_load_dwordx4 v2, s[24:27], s45 offen offset:1024 lds
  v_mfma_f32_16x16x32_bf16 v[88:91], a[8:11], a[40:43], v[88:91]
  ds_read_b64_tr_b16 a[2:3], v5 offset:2720
  v_mfma_f32_16x16x32_bf16 v[92:95], a[8:11], a[44:47], v[92:95]
  buffer_load_dwordx4 v2, s[24:27], s45 offen offset:2048 lds
  v_mfma_f32_16x16x32_bf16 v[108:111], a[12:15], a[44:47], v[108:111]
  ds_read_b64_tr_b16 a[4:5], v5 offset:18624
  v_mfma_f32_16x16x32_bf16 v[104:107], a[12:15], a[40:43], v[104:107]
  buffer_load_dwordx4 v2, s[24:27], s45 offen offset:3072 lds
  v_mfma_f32_16x16x32_bf16 v[100:103], a[12:15], a[36:39], v[100:103]
  ds_read_b64_tr_b16 a[6:7], v5 offset:20928
  v_mfma_f32_16x16x32_bf16 v[96:99], a[12:15], a[32:35], v[96:99]
  s_add_u32 m0, s50, 8192
  s_add_u32 s45, s49, 8192
  s_waitcnt lgkmcnt(3)
  v_mfma_f32_16x16x32_bf16 v[112:115], a[16:19], a[32:35], v[112:115]
  s_waitcnt vmcnt(8)
  s_barrier
  v_mfma_f32_16x16x32_bf16 v[116:119], a[16:19], a[36:39], v[116:119]
  buffer_load_dwordx4 v2, s[24:27], s45 offen lds
  v_mfma_f32_16x16x32_bf16 v[120:123], a[16:19], a[40:43], v[120:123]
  ds_read_b64_tr_b16 a[8:9], v5 offset:18912
  v_mfma_f32_16x16x32_bf16 v[124:127], a[16:19], a[44:47], v[124:127]
  buffer_load_dwordx4 v2, s[24:27], s45 offen offset:1024 lds
  v_mfma_f32_16x16x32_bf16 v[140:143], a[20:23], a[44:47], v[140:143]
  ds_read_b64_tr_b16 a[10:11], v5 offset:21216
  v_mfma_f32_16x16x32_bf16 v[136:139], a[20:23], a[40:43], v[136:139]
  buffer_load_dwordx4 v2, s[24:27], s45 offen offset:1152 lds
  v_mfma_f32_16x16x32_bf16 v[132:135], a[20:23], a[36:39], v[132:135]
  ds_read_b64_tr_b16 a[12:13], v5 offset:37120
  v_mfma_f32_16x16x32_bf16 v[128:131], a[20:23], a[32:35], v[128:131]
  ds_read_b64_tr_b16 a[14:15], v5 offset:39424
  v_mfma_f32_16x16x32_bf16 v[144:147], a[24:27], a[32:35], v[144:147]
  ds_read_b64_tr_b16 a[16:17], v5 offset:37408
  v_mfma_f32_16x16x32_bf16 v[148:151], a[24:27], a[36:39], v[148:151]
  ds_read_b64_tr_b16 a[18:19], v5 offset:39712
  s_waitcnt lgkmcnt(5)
  v_mfma_f32_16x16x32_bf16 v[156:159], a[0:3], a[36:39], v[156:159]
  ds_read_b64_tr_b16 a[20:21], v5 offset:55616
  v_mfma_f32_16x16x32_bf16 v[152:155], a[0:3], a[32:35], v[152:155]
  ds_read_b64_tr_b16 a[22:23], v5 offset:57920
  v_mfma_f32_16x16x32_bf16 v[144:147], a[4:7], a[64:67], v[144:147]
  ds_read_b64_tr_b16 a[24:25], v5 offset:55904
  ds_read_b64_tr_b16 a[26:27], v5 offset:58208
  v_mfma_f32_16x16x32_bf16 v[148:151], a[4:7], a[68:71], v[148:151]
  ds_read_b64_tr_b16 a[0:1], v6 offset:128
  s_waitcnt lgkmcnt(7)
  v_mfma_f32_16x16x32_bf16 v[152:155], a[8:11], a[64:67], v[152:155]
  ds_read_b64_tr_b16 a[2:3], v6 offset:2432
  ds_read_b64_tr_b16 a[4:5], v6 offset:416
  v_mfma_f32_16x16x32_bf16 v[156:159], a[8:11], a[68:71], v[156:159]
  ds_read_b64_tr_b16 a[6:7], v6 offset:2720
  ds_read_b64_tr_b16 a[8:9], v3 offset:18496
  v_mfma_f32_16x16x32_bf16 v[144:147], a[12:15], a[80:83], v[144:147]
  ds_read_b64_tr_b16 a[10:11], v3 offset:20800
  v_mfma_f32_16x16x32_bf16 v[148:151], a[12:15], a[84:87], v[148:151]
  ds_read_b64_tr_b16 a[12:13], v3 offset:18784
  s_waitcnt lgkmcnt(9)
  v_mfma_f32_16x16x32_bf16 v[152:155], a[16:19], a[80:83], v[152:155]
  ds_read_b64_tr_b16 a[14:15], v3 offset:21088
  v_mfma_f32_16x16x32_bf16 v[156:159], a[16:19], a[84:87], v[156:159]
  ds_read_b64_tr_b16 a[16:17], v3 offset:36992
  ds_read_b64_tr_b16 a[18:19], v3 offset:39296
  v_mfma_f32_16x16x32_bf16 v[144:147], a[20:23], a[96:99], v[144:147]
  v_mfma_f32_16x16x32_bf16 v[148:151], a[20:23], a[100:103], v[148:151]
  ds_read_b64_tr_b16 a[20:21], v3 offset:37280
  s_waitcnt lgkmcnt(6)
  v_mfma_f32_16x16x32_bf16 v[152:155], a[24:27], a[96:99], v[152:155]
  ds_read_b64_tr_b16 a[22:23], v3 offset:39584
  v_mfma_f32_16x16x32_bf16 v[156:159], a[24:27], a[100:103], v[156:159]
  ds_read_b64_tr_b16 a[24:25], v3 offset:55488
  ds_read_b64_tr_b16 a[26:27], v3 offset:57792
  v_mfma_f32_16x16x32_bf16 v[144:147], a[0:3], a[112:115], v[144:147]
  v_mfma_f32_16x16x32_bf16 v[148:151], a[0:3], a[116:119], v[148:151]
  v_mfma_f32_16x16x32_bf16 v[152:155], a[4:7], a[112:115], v[152:155]
  s_nop 5
  v_mul_f32_e64 v232, |v144|, s42
  v_mul_f32_e64 v233, |v145|, s42
  v_mul_f32_e64 v234, |v146|, s42
  v_mul_f32_e64 v235, |v147|, s42
  v_mfma_f32_16x16x32_bf16 v[156:159], a[4:7], a[116:119], v[156:159]
  ds_read_b64_tr_b16 a[0:1], v3 offset:55776
  v_mul_f32 v236, s43, v152
  v_mul_f32 v237, s43, v153
  v_mul_f32 v238, s43, v154
  s_waitcnt lgkmcnt(3)
  v_mfma_f32_16x16x32_bf16 v[16:19], a[8:11], a[64:67], v[16:19]
  ds_read_b64_tr_b16 a[2:3], v3 offset:58080
  v_mul_f32 v239, s43, v155
  v_exp_f32 v232, v232
  v_mfma_f32_16x16x32_bf16 v[20:23], a[8:11], a[68:71], v[20:23]
  v_exp_f32 v233, v233
  v_exp_f32 v234, v234
  v_mfma_f32_16x16x32_bf16 v[24:27], a[8:11], a[72:75], v[24:27]
  v_exp_f32 v235, v235
  v_exp_f32 v236, v236
  v_mfma_f32_16x16x32_bf16 v[28:31], a[8:11], a[76:79], v[28:31]
  ds_read_b64_tr_b16 a[4:5], v4
  v_exp_f32 v237, v237
  v_exp_f32 v238, v238
  v_mfma_f32_16x16x32_bf16 v[32:35], a[12:15], a[64:67], v[32:35]
  ds_read_b64_tr_b16 a[6:7], v4 offset:2304
  v_exp_f32 v239, v239
  v_add_f32 v152, 1.0, v232
  v_mfma_f32_16x16x32_bf16 v[36:39], a[12:15], a[68:71], v[36:39]
  v_add_f32 v153, 1.0, v233
  v_add_f32 v154, 1.0, v234
  v_add_f32 v155, 1.0, v235
  v_mfma_f32_16x16x32_bf16 v[40:43], a[12:15], a[72:75], v[40:43]
  v_fma_f32 v236, v152, v236, v152
  v_fma_f32 v237, v153, v237, v153
  v_fma_f32 v238, v154, v238, v154
  v_mfma_f32_16x16x32_bf16 v[44:47], a[12:15], a[76:79], v[44:47]
  ds_read_b64_tr_b16 a[8:9], v4 offset:288
  s_waitcnt vmcnt(0)
  s_barrier
  v_fma_f32 v239, v155, v239, v155
  v_rcp_f32 v236, v236
  v_mfma_f32_16x16x32_bf16 v[16:19], a[16:19], a[80:83], v[16:19]
  ds_read_b64_tr_b16 a[10:11], v4 offset:2592
  v_rcp_f32 v237, v237
  v_rcp_f32 v238, v238
  v_mfma_f32_16x16x32_bf16 v[20:23], a[16:19], a[84:87], v[20:23]
  ds_read_b64_tr_b16 a[232:233], v250
  v_rcp_f32 v239, v239
  v_fma_f32 v232, -v232, v236, v236
  v_mfma_f32_16x16x32_bf16 v[24:27], a[16:19], a[88:91], v[24:27]
  ds_read_b64_tr_b16 a[234:235], v250 offset:32
  v_fma_f32 v233, -v233, v237, v237
  v_fma_f32 v234, -v234, v238, v238
  v_fma_f32 v235, -v235, v239, v239
  v_mfma_f32_16x16x32_bf16 v[28:31], a[16:19], a[92:95], v[28:31]
  ds_read_b64_tr_b16 a[12:13], v3 offset:18528
  v_and_or_b32 v232, v144, s44, v232
  v_and_or_b32 v233, v145, s44, v233
  v_and_or_b32 v234, v146, s44, v234
  v_mfma_f32_16x16x32_bf16 v[32:35], a[20:23], a[80:83], v[32:35]
  ds_read_b64_tr_b16 a[14:15], v3 offset:20832
  v_and_or_b32 v235, v147, s44, v235
  v_cvt_pk_bf16_f32 v144, v232, v233
  v_cvt_pk_bf16_f32 v145, v234, v235
  v_mfma_f32_16x16x32_bf16 v[36:39], a[20:23], a[84:87], v[36:39]
  ds_read_b64_tr_b16 a[236:237], v250 offset:64
  v_mul_f32_e64 v240, |v148|, s42
  v_mul_f32_e64 v241, |v149|, s42
  v_mul_f32_e64 v242, |v150|, s42
  v_mfma_f32_16x16x32_bf16 v[40:43], a[20:23], a[88:91], v[40:43]
  ds_read_b64_tr_b16 a[238:239], v250 offset:96
  v_mul_f32_e64 v243, |v151|, s42
  v_mul_f32 v244, s43, v156
  v_mul_f32 v245, s43, v157
  v_mfma_f32_16x16x32_bf16 v[44:47], a[20:23], a[92:95], v[44:47]
  ds_read_b64_tr_b16 a[16:17], v3 offset:18816
  v_mul_f32 v246, s43, v158
  v_mul_f32 v247, s43, v159
  v_exp_f32 v240, v240
  s_waitcnt lgkmcnt(4)
  v_mfma_f32_16x16x32_bf16 v[16:19], a[24:27], a[96:99], v[16:19]
  ds_read_b64_tr_b16 a[18:19], v3 offset:21120
  v_exp_f32 v241, v241
  v_exp_f32 v242, v242
  v_mfma_f32_16x16x32_bf16 v[20:23], a[24:27], a[100:103], v[20:23]
  ds_read_b64_tr_b16 a[240:241], v250 offset:128
  v_exp_f32 v243, v243
  v_exp_f32 v244, v244
  v_mfma_f32_16x16x32_bf16 v[24:27], a[24:27], a[104:107], v[24:27]
  ds_read_b64_tr_b16 a[242:243], v250 offset:160
  v_exp_f32 v245, v245
  v_exp_f32 v246, v246
  v_mfma_f32_16x16x32_bf16 v[28:31], a[24:27], a[108:111], v[28:31]
  ds_read_b64_tr_b16 a[20:21], v3 offset:37024
  v_exp_f32 v247, v247
  v_mfma_f32_16x16x32_bf16 v[32:35], a[0:3], a[96:99], v[32:35]
  ds_read_b64_tr_b16 a[22:23], v3 offset:39328
  v_add_f32 v156, 1.0, v240
  v_add_f32 v157, 1.0, v241
  v_add_f32 v158, 1.0, v242
  v_mfma_f32_16x16x32_bf16 v[36:39], a[0:3], a[100:103], v[36:39]
  ds_read_b64_tr_b16 a[244:245], v250 offset:192
  v_add_f32 v159, 1.0, v243
  v_fma_f32 v244, v156, v244, v156
  v_fma_f32 v245, v157, v245, v157
  v_mfma_f32_16x16x32_bf16 v[40:43], a[0:3], a[104:107], v[40:43]
  ds_read_b64_tr_b16 a[246:247], v250 offset:224
  v_fma_f32 v246, v158, v246, v158
  v_fma_f32 v247, v159, v247, v159
  v_rcp_f32 v244, v244
  v_mfma_f32_16x16x32_bf16 v[44:47], a[0:3], a[108:111], v[44:47]
  ds_read_b64_tr_b16 a[24:25], v3 offset:37312
  v_rcp_f32 v245, v245
  v_mfma_f32_16x16x32_bf16 v[16:19], a[4:7], a[112:115], v[16:19]
  ds_read_b64_tr_b16 a[26:27], v3 offset:39616
  v_rcp_f32 v246, v246
  v_rcp_f32 v247, v247
  v_mfma_f32_16x16x32_bf16 v[20:23], a[4:7], a[116:119], v[20:23]
  ds_read_b64_tr_b16 a[248:249], v250 offset:256
  v_fma_f32 v240, -v240, v244, v244
  v_fma_f32 v241, -v241, v245, v245
  v_mfma_f32_16x16x32_bf16 v[24:27], a[4:7], a[120:123], v[24:27]
  ds_read_b64_tr_b16 a[196:197], v248
  v_fma_f32 v242, -v242, v246, v246
  v_fma_f32 v243, -v243, v247, v247
  v_mfma_f32_16x16x32_bf16 v[28:31], a[4:7], a[124:127], v[28:31]
  ds_read_b64_tr_b16 a[0:1], v3 offset:55520
  v_and_or_b32 v240, v148, s44, v240
  v_and_or_b32 v241, v149, s44, v241
  v_mfma_f32_16x16x32_bf16 v[32:35], a[8:11], a[112:115], v[32:35]
  ds_read_b64_tr_b16 a[2:3], v3 offset:57824
  v_and_or_b32 v242, v150, s44, v242
  v_and_or_b32 v243, v151, s44, v243
  v_mfma_f32_16x16x32_bf16 v[36:39], a[8:11], a[116:119], v[36:39]
  ds_read_b64_tr_b16 a[198:199], v248 offset:4608
  v_cvt_pk_bf16_f32 v148, v240, v241
  v_cvt_pk_bf16_f32 v149, v242, v243
  v_mfma_f32_16x16x32_bf16 v[40:43], a[8:11], a[120:123], v[40:43]
  ds_read_b64_tr_b16 a[200:201], v248 offset:32
  v_mul_f32_e64 v232, |v24|, s42
  v_mul_f32_e64 v233, |v25|, s42
  v_mul_f32_e64 v234, |v26|, s42
  v_mfma_f32_16x16x32_bf16 v[44:47], a[8:11], a[124:127], v[44:47]
  ds_read_b64_tr_b16 a[4:5], v3 offset:55808
  v_mul_f32_e64 v235, |v27|, s42
  s_waitcnt lgkmcnt(6)
  v_mfma_f32_16x16x32_bf16 v[48:51], a[12:15], a[64:67], v[48:51]
  ds_read_b64_tr_b16 a[6:7], v3 offset:58112
  v_mul_f32 v236, s43, v40
  v_mul_f32 v237, s43, v41
  v_mul_f32 v238, s43, v42
  v_mfma_f32_16x16x32_bf16 v[52:55], a[12:15], a[68:71], v[52:55]
  ds_read_b64_tr_b16 a[202:203], v248 offset:4640
  v_mul_f32 v239, s43, v43
  v_exp_f32 v232, v232
  v_mfma_f32_16x16x32_bf16 v[56:59], a[12:15], a[72:75], v[56:59]
  ds_read_b64_tr_b16 a[204:205], v248 offset:64
  v_exp_f32 v233, v233
  v_exp_f32 v234, v234
  v_mfma_f32_16x16x32_bf16 v[60:63], a[12:15], a[76:79], v[60:63]
  ds_read_b64_tr_b16 a[8:9], v4 offset:32
  v_exp_f32 v235, v235
  v_exp_f32 v236, v236
  v_mfma_f32_16x16x32_bf16 v[64:67], a[16:19], a[64:67], v[64:67]
  ds_read_b64_tr_b16 a[10:11], v4 offset:2336
  v_exp_f32 v237, v237
  v_exp_f32 v238, v238
  v_mfma_f32_16x16x32_bf16 v[68:71], a[16:19], a[68:71], v[68:71]
  ds_read_b64_tr_b16 a[206:207], v248 offset:4672
  v_exp_f32 v239, v239
  v_add_f32 v40, 1.0, v232
  v_mfma_f32_16x16x32_bf16 v[72:75], a[16:19], a[72:75], v[72:75]
  ds_read_b64_tr_b16 a[208:209], v248 offset:96
  v_add_f32 v41, 1.0, v233
  v_add_f32 v42, 1.0, v234
  v_add_f32 v43, 1.0, v235
  v_mfma_f32_16x16x32_bf16 v[76:79], a[16:19], a[76:79], v[76:79]
  ds_read_b64_tr_b16 a[12:13], v4 offset:320
  v_fma_f32 v236, v40, v236, v40
  v_fma_f32 v237, v41, v237, v41
  v_fma_f32 v238, v42, v238, v42
  v_mfma_f32_16x16x32_bf16 v[48:51], a[20:23], a[80:83], v[48:51]
  ds_read_b64_tr_b16 a[14:15], v4 offset:2624
  v_fma_f32 v239, v43, v239, v43
  v_rcp_f32 v236, v236
  v_mfma_f32_16x16x32_bf16 v[52:55], a[20:23], a[84:87], v[52:55]
  ds_read_b64_tr_b16 a[210:211], v248 offset:4704
  v_rcp_f32 v237, v237
  v_rcp_f32 v238, v238
  v_mfma_f32_16x16x32_bf16 v[56:59], a[20:23], a[88:91], v[56:59]
  ds_read_b64_tr_b16 a[212:213], v248 offset:128
  v_rcp_f32 v239, v239
  v_fma_f32 v232, -v232, v236, v236
  v_mfma_f32_16x16x32_bf16 v[60:63], a[20:23], a[92:95], v[60:63]
  ds_read_b64_tr_b16 a[16:17], v3 offset:18560
  v_fma_f32 v233, -v233, v237, v237
  v_fma_f32 v234, -v234, v238, v238
  v_fma_f32 v235, -v235, v239, v239
  v_mfma_f32_16x16x32_bf16 v[64:67], a[24:27], a[80:83], v[64:67]
  ds_read_b64_tr_b16 a[18:19], v3 offset:20864
  v_and_or_b32 v232, v24, s44, v232
  v_and_or_b32 v233, v25, s44, v233
  v_and_or_b32 v234, v26, s44, v234
  v_mfma_f32_16x16x32_bf16 v[68:71], a[24:27], a[84:87], v[68:71]
  ds_read_b64_tr_b16 a[214:215], v248 offset:4736
  v_and_or_b32 v235, v27, s44, v235
  v_cvt_pk_bf16_f32 v24, v232, v233
  v_cvt_pk_bf16_f32 v25, v234, v235
  v_mfma_f32_16x16x32_bf16 v[72:75], a[24:27], a[88:91], v[72:75]
  ds_read_b64_tr_b16 a[216:217], v248 offset:160
  v_mul_f32_e64 v240, |v16|, s42
  v_mul_f32_e64 v241, |v17|, s42
  v_mul_f32_e64 v242, |v18|, s42
  v_mfma_f32_16x16x32_bf16 v[76:79], a[24:27], a[92:95], v[76:79]
  ds_read_b64_tr_b16 a[20:21], v3 offset:18848
  v_mul_f32_e64 v243, |v19|, s42
  v_mul_f32 v244, s43, v32
  v_mul_f32 v245, s43, v33
  s_waitcnt lgkmcnt(4)
  v_mfma_f32_16x16x32_bf16 v[48:51], a[0:3], a[96:99], v[48:51]
  ds_read_b64_tr_b16 a[22:23], v3 offset:21152
  v_mul_f32 v246, s43, v34
  v_mul_f32 v247, s43, v35
  v_exp_f32 v240, v240
  v_mfma_f32_16x16x32_bf16 v[52:55], a[0:3], a[100:103], v[52:55]
  ds_read_b64_tr_b16 a[218:219], v248 offset:4768
  v_exp_f32 v241, v241
  v_exp_f32 v242, v242
  v_mfma_f32_16x16x32_bf16 v[56:59], a[0:3], a[104:107], v[56:59]
  ds_read_b64_tr_b16 a[220:221], v248 offset:192
  v_exp_f32 v243, v243
  v_exp_f32 v244, v244
  v_mfma_f32_16x16x32_bf16 v[60:63], a[0:3], a[108:111], v[60:63]
  ds_read_b64_tr_b16 a[24:25], v3 offset:37056
  v_exp_f32 v245, v245
  v_exp_f32 v246, v246
  v_mfma_f32_16x16x32_bf16 v[64:67], a[4:7], a[96:99], v[64:67]
  ds_read_b64_tr_b16 a[26:27], v3 offset:39360
  v_exp_f32 v247, v247
  v_add_f32 v32, 1.0, v240
  v_mfma_f32_16x16x32_bf16 v[68:71], a[4:7], a[100:103], v[68:71]
  ds_read_b64_tr_b16 a[222:223], v248 offset:4800
  v_add_f32 v33, 1.0, v241
  v_add_f32 v34, 1.0, v242
  v_add_f32 v35, 1.0, v243
  v_mfma_f32_16x16x32_bf16 v[72:75], a[4:7], a[104:107], v[72:75]
  ds_read_b64_tr_b16 a[224:225], v248 offset:224
  v_fma_f32 v244, v32, v244, v32
  v_fma_f32 v245, v33, v245, v33
  v_fma_f32 v246, v34, v246, v34
  v_mfma_f32_16x16x32_bf16 v[76:79], a[4:7], a[108:111], v[76:79]
  ds_read_b64_tr_b16 a[0:1], v3 offset:37344
  v_fma_f32 v247, v35, v247, v35
  v_rcp_f32 v244, v244
  v_mfma_f32_16x16x32_bf16 v[48:51], a[8:11], a[112:115], v[48:51]
  ds_read_b64_tr_b16 a[2:3], v3 offset:39648
  v_rcp_f32 v245, v245
  v_rcp_f32 v246, v246
  v_mfma_f32_16x16x32_bf16 v[52:55], a[8:11], a[116:119], v[52:55]
  ds_read_b64_tr_b16 a[226:227], v248 offset:4832
  v_rcp_f32 v247, v247
  v_fma_f32 v240, -v240, v244, v244
  v_mfma_f32_16x16x32_bf16 v[56:59], a[8:11], a[120:123], v[56:59]
  ds_read_b64_tr_b16 a[228:229], v248 offset:256
  v_fma_f32 v241, -v241, v245, v245
  v_fma_f32 v242, -v242, v246, v246
  v_mfma_f32_16x16x32_bf16 v[60:63], a[8:11], a[124:127], v[60:63]
  ds_read_b64_tr_b16 a[4:5], v3 offset:55552
  v_fma_f32 v243, -v243, v247, v247
  v_and_or_b32 v240, v16, s44, v240
  v_mfma_f32_16x16x32_bf16 v[64:67], a[12:15], a[112:115], v[64:67]
  ds_read_b64_tr_b16 a[6:7], v3 offset:57856
  v_and_or_b32 v241, v17, s44, v241
  v_and_or_b32 v242, v18, s44, v242
  v_mfma_f32_16x16x32_bf16 v[68:71], a[12:15], a[116:119], v[68:71]
  ds_read_b64_tr_b16 a[230:231], v248 offset:4864
  v_and_or_b32 v243, v19, s44, v243
  v_cvt_pk_bf16_f32 v16, v240, v241
  v_mfma_f32_16x16x32_bf16 v[72:75], a[12:15], a[120:123], v[72:75]
  ds_read_b64_tr_b16 a[28:29], v249
  v_cvt_pk_bf16_f32 v17, v242, v243
  v_mul_f32_e64 v232, |v56|, s42
  v_mul_f32_e64 v233, |v57|, s42
  v_mfma_f32_16x16x32_bf16 v[76:79], a[12:15], a[124:127], v[76:79]
  ds_read_b64_tr_b16 a[8:9], v3 offset:55840
  v_mul_f32_e64 v234, |v58|, s42
  v_mul_f32_e64 v235, |v59|, s42
  v_mul_f32 v236, s43, v72
  s_waitcnt lgkmcnt(5)
  v_mfma_f32_16x16x32_bf16 v[80:83], a[16:19], a[64:67], v[80:83]
  ds_read_b64_tr_b16 a[10:11], v3 offset:58144
  v_mul_f32 v237, s43, v73
  v_mul_f32 v238, s43, v74
  v_mul_f32 v239, s43, v75
  v_mfma_f32_16x16x32_bf16 v[84:87], a[16:19], a[68:71], v[84:87]
  ds_read_b64_tr_b16 a[30:31], v249 offset:4608
  v_exp_f32 v232, v232
  v_exp_f32 v233, v233
  v_mfma_f32_16x16x32_bf16 v[88:91], a[16:19], a[72:75], v[88:91]
  ds_read_b64_tr_b16 a[32:33], v249 offset:32
  v_exp_f32 v234, v234
  v_exp_f32 v235, v235
  v_mfma_f32_16x16x32_bf16 v[92:95], a[16:19], a[76:79], v[92:95]
  ds_read_b64_tr_b16 a[12:13], v4 offset:64
  v_exp_f32 v236, v236
  v_exp_f32 v237, v237
  v_mfma_f32_16x16x32_bf16 v[96:99], a[20:23], a[64:67], v[96:99]
  ds_read_b64_tr_b16 a[14:15], v4 offset:2368
  v_exp_f32 v238, v238
  v_exp_f32 v239, v239
  v_mfma_f32_16x16x32_bf16 v[100:103], a[20:23], a[68:71], v[100:103]
  ds_read_b64_tr_b16 a[34:35], v249 offset:4640
  v_add_f32 v72, 1.0, v232
  v_add_f32 v73, 1.0, v233
  v_add_f32 v74, 1.0, v234
  v_mfma_f32_16x16x32_bf16 v[104:107], a[20:23], a[72:75], v[104:107]
  ds_read_b64_tr_b16 a[36:37], v249 offset:64
  v_add_f32 v75, 1.0, v235
  v_fma_f32 v236, v72, v236, v72
  v_fma_f32 v237, v73, v237, v73
  v_mfma_f32_16x16x32_bf16 v[108:111], a[20:23], a[76:79], v[108:111]
  ds_read_b64_tr_b16 a[16:17], v4 offset:352
  v_fma_f32 v238, v74, v238, v74
  v_fma_f32 v239, v75, v239, v75
  v_rcp_f32 v236, v236
  v_mfma_f32_16x16x32_bf16 v[80:83], a[24:27], a[80:83], v[80:83]
  ds_read_b64_tr_b16 a[18:19], v4 offset:2656
  v_rcp_f32 v237, v237
  v_rcp_f32 v238, v238
  v_mfma_f32_16x16x32_bf16 v[84:87], a[24:27], a[84:87], v[84:87]
  ds_read_b64_tr_b16 a[38:39], v249 offset:4672
  v_rcp_f32 v239, v239
  v_fma_f32 v232, -v232, v236, v236
  v_mfma_f32_16x16x32_bf16 v[88:91], a[24:27], a[88:91], v[88:91]
  ds_read_b64_tr_b16 a[40:41], v249 offset:96
  v_fma_f32 v233, -v233, v237, v237
  v_fma_f32 v234, -v234, v238, v238
  v_fma_f32 v235, -v235, v239, v239
  v_mfma_f32_16x16x32_bf16 v[92:95], a[24:27], a[92:95], v[92:95]
  ds_read_b64_tr_b16 a[20:21], v3 offset:18592
  v_and_or_b32 v232, v56, s44, v232
  v_and_or_b32 v233, v57, s44, v233
  v_and_or_b32 v234, v58, s44, v234
  v_mfma_f32_16x16x32_bf16 v[96:99], a[0:3], a[80:83], v[96:99]
  ds_read_b64_tr_b16 a[22:23], v3 offset:20896
  v_and_or_b32 v235, v59, s44, v235
  v_cvt_pk_bf16_f32 v26, v232, v233
  v_cvt_pk_bf16_f32 v27, v234, v235
  v_mfma_f32_16x16x32_bf16 v[100:103], a[0:3], a[84:87], v[100:103]
  ds_write_b128 v251, v[24:27]
  v_mul_f32_e64 v240, |v48|, s42
  v_mul_f32_e64 v241, |v49|, s42
  v_mul_f32_e64 v242, |v50|, s42
  v_mfma_f32_16x16x32_bf16 v[104:107], a[0:3], a[88:91], v[104:107]
  ds_read_b64_tr_b16 a[42:43], v249 offset:4704
  v_mul_f32_e64 v243, |v51|, s42
  v_mul_f32 v244, s43, v64
  v_mul_f32 v245, s43, v65
  v_mfma_f32_16x16x32_bf16 v[108:111], a[0:3], a[92:95], v[108:111]
  ds_read_b64_tr_b16 a[24:25], v3 offset:18880
  v_mul_f32 v246, s43, v66
  v_mul_f32 v247, s43, v67
  v_exp_f32 v240, v240
  s_waitcnt lgkmcnt(4)
  v_mfma_f32_16x16x32_bf16 v[80:83], a[4:7], a[96:99], v[80:83]
  ds_read_b64_tr_b16 a[26:27], v3 offset:21184
  v_exp_f32 v241, v241
  v_exp_f32 v242, v242
  v_mfma_f32_16x16x32_bf16 v[84:87], a[4:7], a[100:103], v[84:87]
  ds_read_b64_tr_b16 a[44:45], v249 offset:128
  v_exp_f32 v243, v243
  v_exp_f32 v244, v244
  v_mfma_f32_16x16x32_bf16 v[88:91], a[4:7], a[104:107], v[88:91]
  ds_read_b64_tr_b16 a[46:47], v249 offset:4736
  v_exp_f32 v245, v245
  v_exp_f32 v246, v246
  v_mfma_f32_16x16x32_bf16 v[92:95], a[4:7], a[108:111], v[92:95]
  ds_read_b64_tr_b16 a[0:1], v3 offset:37088
  v_exp_f32 v247, v247
  v_mfma_f32_16x16x32_bf16 v[96:99], a[8:11], a[96:99], v[96:99]
  ds_read_b64_tr_b16 a[2:3], v3 offset:39392
  v_add_f32 v64, 1.0, v240
  v_add_f32 v65, 1.0, v241
  v_add_f32 v66, 1.0, v242
  v_mfma_f32_16x16x32_bf16 v[100:103], a[8:11], a[100:103], v[100:103]
  ds_read_b64_tr_b16 a[48:49], v249 offset:160
  v_add_f32 v67, 1.0, v243
  v_fma_f32 v244, v64, v244, v64
  v_fma_f32 v245, v65, v245, v65
  v_mfma_f32_16x16x32_bf16 v[104:107], a[8:11], a[104:107], v[104:107]
  ds_read_b64_tr_b16 a[50:51], v249 offset:4768
  v_fma_f32 v246, v66, v246, v66
  v_fma_f32 v247, v67, v247, v67
  v_rcp_f32 v244, v244
  v_mfma_f32_16x16x32_bf16 v[108:111], a[8:11], a[108:111], v[108:111]
  ds_read_b64_tr_b16 a[4:5], v3 offset:37376
  v_rcp_f32 v245, v245
  v_mfma_f32_16x16x32_bf16 v[80:83], a[12:15], a[112:115], v[80:83]
  ds_read_b64_tr_b16 a[6:7], v3 offset:39680
  v_rcp_f32 v246, v246
  v_rcp_f32 v247, v247
  v_mfma_f32_16x16x32_bf16 v[84:87], a[12:15], a[116:119], v[84:87]
  ds_read_b64_tr_b16 a[52:53], v249 offset:192
  v_fma_f32 v240, -v240, v244, v244
  v_fma_f32 v241, -v241, v245, v245
  v_mfma_f32_16x16x32_bf16 v[88:91], a[12:15], a[120:123], v[88:91]
  ds_read_b64_tr_b16 a[54:55], v249 offset:4800
  v_fma_f32 v242, -v242, v246, v246
  v_fma_f32 v243, -v243, v247, v247
  v_mfma_f32_16x16x32_bf16 v[92:95], a[12:15], a[124:127], v[92:95]
  ds_read_b64_tr_b16 a[8:9], v3 offset:55584
  v_and_or_b32 v240, v48, s44, v240
  v_and_or_b32 v241, v49, s44, v241
  v_mfma_f32_16x16x32_bf16 v[96:99], a[16:19], a[112:115], v[96:99]
  ds_read_b64_tr_b16 a[10:11], v3 offset:57888
  v_and_or_b32 v242, v50, s44, v242
  v_and_or_b32 v243, v51, s44, v243
  v_mfma_f32_16x16x32_bf16 v[100:103], a[16:19], a[116:119], v[100:103]
  ds_read_b64_tr_b16 a[56:57], v249 offset:224
  v_cvt_pk_bf16_f32 v18, v240, v241
  v_cvt_pk_bf16_f32 v19, v242, v243
  v_mfma_f32_16x16x32_bf16 v[104:107], a[16:19], a[120:123], v[104:107]
  ds_read_b64_tr_b16 a[58:59], v249 offset:4832
  v_mul_f32_e64 v232, |v88|, s42
  v_mul_f32_e64 v233, |v89|, s42
  v_mul_f32_e64 v234, |v90|, s42
  v_mfma_f32_16x16x32_bf16 v[108:111], a[16:19], a[124:127], v[108:111]
  ds_read_b64_tr_b16 a[12:13], v3 offset:55872
  v_mul_f32_e64 v235, |v91|, s42
  s_waitcnt lgkmcnt(15)
  v_mfma_f32_16x16x32_bf16 v[112:115], a[20:23], a[64:67], v[112:115]
  ds_read_b64_tr_b16 a[14:15], v3 offset:58176
  v_mul_f32 v236, s43, v104
  v_mul_f32 v237, s43, v105
  v_mul_f32 v238, s43, v106
  v_mfma_f32_16x16x32_bf16 v[116:119], a[20:23], a[68:71], v[116:119]
  ds_read_b64_tr_b16 a[60:61], v249 offset:256
  v_mul_f32 v239, s43, v107
  v_exp_f32 v232, v232
  v_mfma_f32_16x16x32_bf16 v[120:123], a[20:23], a[72:75], v[120:123]
  ds_read_b64_tr_b16 a[62:63], v249 offset:4864
  v_exp_f32 v233, v233
  v_exp_f32 v234, v234
  v_mfma_f32_16x16x32_bf16 v[124:127], a[20:23], a[76:79], v[124:127]
  ds_read_b64_tr_b16 a[16:17], v4 offset:96
  v_exp_f32 v235, v235
  v_exp_f32 v236, v236
  v_mfma_f32_16x16x32_bf16 v[128:131], a[24:27], a[64:67], v[128:131]
  ds_read_b64_tr_b16 a[18:19], v4 offset:2400
  v_exp_f32 v237, v237
  v_exp_f32 v238, v238
  v_mfma_f32_16x16x32_bf16 v[132:135], a[24:27], a[68:71], v[132:135]
  ds_read_b64_tr_b16 a[160:161], v248 offset:9216
  v_exp_f32 v239, v239
  v_add_f32 v104, 1.0, v232
  v_mfma_f32_16x16x32_bf16 v[136:139], a[24:27], a[72:75], v[136:139]
  ds_read_b64_tr_b16 a[162:163], v248 offset:13824
  v_add_f32 v105, 1.0, v233
  v_add_f32 v106, 1.0, v234
  v_add_f32 v107, 1.0, v235
  v_mfma_f32_16x16x32_bf16 v[140:143], a[24:27], a[76:79], v[140:143]
  ds_read_b64_tr_b16 a[20:21], v4 offset:384
  v_fma_f32 v236, v104, v236, v104
  v_fma_f32 v237, v105, v237, v105
  v_fma_f32 v238, v106, v238, v106
  s_waitcnt lgkmcnt(4)
  v_mfma_f32_16x16x32_bf16 v[112:115], a[0:3], a[80:83], v[112:115]
  ds_read_b64_tr_b16 a[22:23], v4 offset:2688
  v_fma_f32 v239, v107, v239, v107
  v_rcp_f32 v236, v236
  v_mfma_f32_16x16x32_bf16 v[116:119], a[0:3], a[84:87], v[116:119]
  ds_read_b64_tr_b16 a[164:165], v248 offset:9248
  v_rcp_f32 v237, v237
  v_rcp_f32 v238, v238
  v_mfma_f32_16x16x32_bf16 v[120:123], a[0:3], a[88:91], v[120:123]
  ds_read_b64_tr_b16 a[166:167], v248 offset:13856
  v_rcp_f32 v239, v239
  v_fma_f32 v232, -v232, v236, v236
  v_mfma_f32_16x16x32_bf16 v[124:127], a[0:3], a[92:95], v[124:127]
  ds_read_b64_tr_b16 a[168:169], v248 offset:9280
  v_fma_f32 v233, -v233, v237, v237
  v_fma_f32 v234, -v234, v238, v238
  v_fma_f32 v235, -v235, v239, v239
  v_mfma_f32_16x16x32_bf16 v[128:131], a[4:7], a[80:83], v[128:131]
  ds_read_b64_tr_b16 a[170:171], v248 offset:13888
  v_and_or_b32 v232, v88, s44, v232
  v_and_or_b32 v233, v89, s44, v233
  v_and_or_b32 v234, v90, s44, v234
  v_mfma_f32_16x16x32_bf16 v[132:135], a[4:7], a[84:87], v[132:135]
  ds_read_b64_tr_b16 a[172:173], v248 offset:9312
  v_and_or_b32 v235, v91, s44, v235
  v_cvt_pk_bf16_f32 v88, v232, v233
  v_cvt_pk_bf16_f32 v89, v234, v235
  v_mfma_f32_16x16x32_bf16 v[136:139], a[4:7], a[88:91], v[136:139]
  ds_read_b64_tr_b16 a[174:175], v248 offset:13920
  v_mul_f32_e64 v240, |v80|, s42
  v_mul_f32_e64 v241, |v81|, s42
  v_mul_f32_e64 v242, |v82|, s42
  v_mfma_f32_16x16x32_bf16 v[140:143], a[4:7], a[92:95], v[140:143]
  ds_read_b64_tr_b16 a[176:177], v248 offset:9344
  v_mul_f32_e64 v243, |v83|, s42
  v_mul_f32 v244, s43, v96
  v_mul_f32 v245, s43, v97
  v_mfma_f32_16x16x32_bf16 v[112:115], a[8:11], a[96:99], v[112:115]
  ds_read_b64_tr_b16 a[178:179], v248 offset:13952
  v_mul_f32 v246, s43, v98
  v_mul_f32 v247, s43, v99
  v_exp_f32 v240, v240
  v_mfma_f32_16x16x32_bf16 v[116:119], a[8:11], a[100:103], v[116:119]
  ds_read_b64_tr_b16 a[180:181], v248 offset:9376
  v_exp_f32 v241, v241
  v_exp_f32 v242, v242
  v_mfma_f32_16x16x32_bf16 v[120:123], a[8:11], a[104:107], v[120:123]
  ds_read_b64_tr_b16 a[182:183], v248 offset:13984
  v_exp_f32 v243, v243
  v_exp_f32 v244, v244
  v_mfma_f32_16x16x32_bf16 v[124:127], a[8:11], a[108:111], v[124:127]
  ds_read_b64_tr_b16 a[184:185], v248 offset:9408
  v_exp_f32 v245, v245
  v_exp_f32 v246, v246
  v_mfma_f32_16x16x32_bf16 v[128:131], a[12:15], a[96:99], v[128:131]
  ds_read_b64_tr_b16 a[186:187], v248 offset:14016
  v_exp_f32 v247, v247
  v_add_f32 v96, 1.0, v240
  v_mfma_f32_16x16x32_bf16 v[132:135], a[12:15], a[100:103], v[132:135]
  ds_read_b64_tr_b16 a[188:189], v248 offset:9440
  v_add_f32 v97, 1.0, v241
  v_add_f32 v98, 1.0, v242
  v_add_f32 v99, 1.0, v243
  v_mfma_f32_16x16x32_bf16 v[136:139], a[12:15], a[104:107], v[136:139]
  ds_read_b64_tr_b16 a[190:191], v248 offset:14048
  v_fma_f32 v244, v96, v244, v96
  v_fma_f32 v245, v97, v245, v97
  v_fma_f32 v246, v98, v246, v98
  v_mfma_f32_16x16x32_bf16 v[140:143], a[12:15], a[108:111], v[140:143]
  ds_read_b64_tr_b16 a[192:193], v248 offset:9472
  v_fma_f32 v247, v99, v247, v99
  v_rcp_f32 v244, v244
  s_waitcnt lgkmcnt(4)
  v_mfma_f32_16x16x32_bf16 v[112:115], a[16:19], a[112:115], v[112:115]
  ds_read_b64_tr_b16 a[194:195], v248 offset:14080
  v_rcp_f32 v245, v245
  v_rcp_f32 v246, v246
  v_mfma_f32_16x16x32_bf16 v[116:119], a[16:19], a[116:119], v[116:119]
  v_rcp_f32 v247, v247
  v_fma_f32 v240, -v240, v244, v244
  v_mfma_f32_16x16x32_bf16 v[120:123], a[16:19], a[120:123], v[120:123]
  v_fma_f32 v241, -v241, v245, v245
  v_fma_f32 v242, -v242, v246, v246
  v_mfma_f32_16x16x32_bf16 v[124:127], a[16:19], a[124:127], v[124:127]
  v_fma_f32 v243, -v243, v247, v247
  v_and_or_b32 v240, v80, s44, v240
  v_mfma_f32_16x16x32_bf16 v[128:131], a[20:23], a[112:115], v[128:131]
  v_and_or_b32 v241, v81, s44, v241
  v_and_or_b32 v242, v82, s44, v242
  v_mfma_f32_16x16x32_bf16 v[132:135], a[20:23], a[116:119], v[132:135]
  v_and_or_b32 v243, v83, s44, v243
  v_cvt_pk_bf16_f32 v80, v240, v241
  v_mfma_f32_16x16x32_bf16 v[136:139], a[20:23], a[120:123], v[136:139]
  v_cvt_pk_bf16_f32 v81, v242, v243
  v_mul_f32_e64 v232, |v120|, s42
  v_mul_f32_e64 v233, |v121|, s42
  v_mul_f32_e64 v234, |v122|, s42
  v_mul_f32_e64 v235, |v123|, s42
  v_mfma_f32_16x16x32_bf16 v[140:143], a[20:23], a[124:127], v[140:143]
  ds_read_b64_tr_b16 a[64:65], v249 offset:9216
  s_nop 0
  v_mul_f32 v236, s43, v136
  v_mul_f32 v237, s43, v137
  v_mul_f32 v238, s43, v138
  v_mul_f32 v239, s43, v139
  v_exp_f32 v232, v232
  v_exp_f32 v233, v233
  v_mfma_f32_16x16x16_bf16 v[160:163], a[232:233], v[144:145], 0
  ds_read_b64_tr_b16 a[66:67], v249 offset:13824
  v_exp_f32 v234, v234
  v_exp_f32 v235, v235
  v_exp_f32 v236, v236
  v_exp_f32 v237, v237
  v_mfma_f32_16x16x16_bf16 v[164:167], a[232:233], v[148:149], 0
  ds_read_b64_tr_b16 a[68:69], v249 offset:9248
  v_exp_f32 v238, v238
  v_exp_f32 v239, v239
  v_add_f32 v136, 1.0, v232
  v_add_f32 v137, 1.0, v233
  v_add_f32 v138, 1.0, v234
  v_mfma_f32_16x16x16_bf16 v[168:171], a[234:235], v[144:145], 0
  ds_read_b64_tr_b16 a[70:71], v249 offset:13856
  v_add_f32 v139, 1.0, v235
  v_fma_f32 v236, v136, v236, v136
  v_fma_f32 v237, v137, v237, v137
  v_fma_f32 v238, v138, v238, v138
  v_fma_f32 v239, v139, v239, v139
  v_rcp_f32 v236, v236
  v_mfma_f32_16x16x16_bf16 v[172:175], a[234:235], v[148:149], 0
  ds_read_b64_tr_b16 a[72:73], v249 offset:9280
  v_rcp_f32 v237, v237
  v_rcp_f32 v238, v238
  v_rcp_f32 v239, v239
  v_fma_f32 v232, -v232, v236, v236
  v_mfma_f32_16x16x16_bf16 v[176:179], a[236:237], v[144:145], 0
  ds_read_b64_tr_b16 a[74:75], v249 offset:13888
  v_fma_f32 v233, -v233, v237, v237
  v_fma_f32 v234, -v234, v238, v238
  v_fma_f32 v235, -v235, v239, v239
  v_and_or_b32 v232, v120, s44, v232
  v_and_or_b32 v233, v121, s44, v233
  v_and_or_b32 v234, v122, s44, v234
  v_and_or_b32 v235, v123, s44, v235
  v_mfma_f32_16x16x16_bf16 v[180:183], a[236:237], v[148:149], 0
  ds_read_b64_tr_b16 a[76:77], v249 offset:9312
  v_cvt_pk_bf16_f32 v90, v232, v233
  v_cvt_pk_bf16_f32 v91, v234, v235
  v_mul_f32_e64 v240, |v112|, s42
  v_mul_f32_e64 v241, |v113|, s42
  v_mul_f32_e64 v242, |v114|, s42
  v_mul_f32_e64 v243, |v115|, s42
  v_mul_f32 v244, s43, v128
  v_mfma_f32_16x16x16_bf16 v[184:187], a[238:239], v[144:145], 0
  ds_write_b128 v251, v[88:91] offset:2048
  v_mul_f32 v245, s43, v129
  v_mul_f32 v246, s43, v130
  v_mul_f32 v247, s43, v131
  v_exp_f32 v240, v240
  v_exp_f32 v241, v241
  v_mfma_f32_16x16x16_bf16 v[188:191], a[238:239], v[148:149], 0
  ds_read_b64_tr_b16 a[78:79], v249 offset:13920
  v_exp_f32 v242, v242
  v_exp_f32 v243, v243
  v_exp_f32 v244, v244
  v_exp_f32 v245, v245
  v_mfma_f32_16x16x16_bf16 v[192:195], a[240:241], v[144:145], 0
  ds_read_b64_tr_b16 a[80:81], v249 offset:9344
  s_waitcnt lgkmcnt(2)
  s_barrier
  v_exp_f32 v246, v246
  v_exp_f32 v247, v247
  v_add_f32 v128, 1.0, v240
  v_add_f32 v129, 1.0, v241
  v_add_f32 v130, 1.0, v242
  v_mfma_f32_16x16x16_bf16 v[196:199], a[240:241], v[148:149], 0
  ds_read_b128 a[144:147], v252
  v_add_f32 v131, 1.0, v243
  v_fma_f32 v244, v128, v244, v128
  v_fma_f32 v245, v129, v245, v129
  v_fma_f32 v246, v130, v246, v130
  v_fma_f32 v247, v131, v247, v131
  v_rcp_f32 v244, v244
  v_mfma_f32_16x16x16_bf16 v[200:203], a[242:243], v[144:145], 0
  ds_read_b128 a[152:155], v252 offset:2048
  v_rcp_f32 v245, v245
  v_rcp_f32 v246, v246
  v_rcp_f32 v247, v247
  v_fma_f32 v240, -v240, v244, v244
  v_mfma_f32_16x16x16_bf16 v[204:207], a[242:243], v[148:149], 0
  ds_read_b64_tr_b16 a[82:83], v249 offset:13952
  v_fma_f32 v241, -v241, v245, v245
  v_fma_f32 v242, -v242, v246, v246
  v_fma_f32 v243, -v243, v247, v247
  v_and_or_b32 v240, v112, s44, v240
  v_and_or_b32 v241, v113, s44, v241
  v_and_or_b32 v242, v114, s44, v242
  v_and_or_b32 v243, v115, s44, v243
  v_mfma_f32_16x16x16_bf16 v[208:211], a[244:245], v[144:145], 0
  ds_read_b64_tr_b16 a[84:85], v249 offset:9376
  v_cvt_pk_bf16_f32 v82, v240, v241
  v_cvt_pk_bf16_f32 v83, v242, v243
  v_mul_f32_e64 v232, |v28|, s42
  v_mul_f32_e64 v233, |v29|, s42
  v_mul_f32_e64 v234, |v30|, s42
  v_mul_f32_e64 v235, |v31|, s42
  v_mul_f32 v236, s43, v44
  v_mfma_f32_16x16x16_bf16 v[212:215], a[244:245], v[148:149], 0
  ds_read_b64_tr_b16 a[86:87], v249 offset:13984
  v_mul_f32 v237, s43, v45
  v_mul_f32 v238, s43, v46
  v_mul_f32 v239, s43, v47
  v_exp_f32 v232, v232
  v_exp_f32 v233, v233
  v_mfma_f32_16x16x16_bf16 v[216:219], a[246:247], v[144:145], 0
  ds_read_b64_tr_b16 a[88:89], v249 offset:9408
  v_exp_f32 v234, v234
  v_exp_f32 v235, v235
  v_exp_f32 v236, v236
  v_exp_f32 v237, v237
  v_mfma_f32_16x16x16_bf16 v[220:223], a[246:247], v[148:149], 0
  ds_read_b64_tr_b16 a[90:91], v249 offset:14016
  v_exp_f32 v238, v238
  v_exp_f32 v239, v239
  v_add_f32 v44, 1.0, v232
  v_add_f32 v45, 1.0, v233
  v_add_f32 v46, 1.0, v234
  v_mfma_f32_16x16x16_bf16 v[224:227], a[248:249], v[144:145], 0
  ds_read_b64_tr_b16 a[92:93], v249 offset:9440
  v_add_f32 v47, 1.0, v235
  v_fma_f32 v236, v44, v236, v44
  v_fma_f32 v237, v45, v237, v45
  v_fma_f32 v238, v46, v238, v46
  v_fma_f32 v239, v47, v239, v47
  v_rcp_f32 v236, v236
  v_mfma_f32_16x16x16_bf16 v[228:231], a[248:249], v[148:149], 0
  ds_read_b64_tr_b16 a[94:95], v249 offset:14048
  v_rcp_f32 v237, v237
  v_rcp_f32 v238, v238
  v_rcp_f32 v239, v239
  v_fma_f32 v232, -v232, v236, v236
  v_mfma_f32_16x16x32_bf16 v[160:163], a[196:199], v[16:19], v[160:163]
  ds_read_b64_tr_b16 a[96:97], v249 offset:9472
  v_fma_f32 v233, -v233, v237, v237
  v_fma_f32 v234, -v234, v238, v238
  v_fma_f32 v235, -v235, v239, v239
  v_and_or_b32 v232, v28, s44, v232
  v_and_or_b32 v233, v29, s44, v233
  v_and_or_b32 v234, v30, s44, v234
  v_and_or_b32 v235, v31, s44, v235
  v_mfma_f32_16x16x32_bf16 v[168:171], a[200:203], v[16:19], v[168:171]
  ds_read_b64_tr_b16 a[98:99], v249 offset:14080
  v_cvt_pk_bf16_f32 v28, v232, v233
  v_cvt_pk_bf16_f32 v29, v234, v235
  v_mul_f32_e64 v240, |v60|, s42
  v_mul_f32_e64 v241, |v61|, s42
  v_mul_f32_e64 v242, |v62|, s42
  v_mul_f32_e64 v243, |v63|, s42
  v_mul_f32 v244, s43, v76
  v_mfma_f32_16x16x32_bf16 v[176:179], a[204:207], v[16:19], v[176:179]
  v_mul_f32 v245, s43, v77
  v_mul_f32 v246, s43, v78
  v_mul_f32 v247, s43, v79
  v_exp_f32 v240, v240
  v_exp_f32 v241, v241
  v_mfma_f32_16x16x32_bf16 v[184:187], a[208:211], v[16:19], v[184:187]
  v_exp_f32 v242, v242
  v_exp_f32 v243, v243
  v_exp_f32 v244, v244
  v_exp_f32 v245, v245
  v_mfma_f32_16x16x32_bf16 v[192:195], a[212:215], v[16:19], v[192:195]
  v_exp_f32 v246, v246
  v_exp_f32 v247, v247
  v_add_f32 v76, 1.0, v240
  v_add_f32 v77, 1.0, v241
  v_add_f32 v78, 1.0, v242
  v_mfma_f32_16x16x32_bf16 v[200:203], a[216:219], v[16:19], v[200:203]
  v_add_f32 v79, 1.0, v243
  v_fma_f32 v244, v76, v244, v76
  v_fma_f32 v245, v77, v245, v77
  v_fma_f32 v246, v78, v246, v78
  v_fma_f32 v247, v79, v247, v79
  v_rcp_f32 v244, v244
  v_mfma_f32_16x16x32_bf16 v[208:211], a[220:223], v[16:19], v[208:211]
  v_rcp_f32 v245, v245
  v_rcp_f32 v246, v246
  v_rcp_f32 v247, v247
  v_fma_f32 v240, -v240, v244, v244
  v_mfma_f32_16x16x32_bf16 v[216:219], a[224:227], v[16:19], v[216:219]
  v_fma_f32 v241, -v241, v245, v245
  v_fma_f32 v242, -v242, v246, v246
  v_fma_f32 v243, -v243, v247, v247
  v_and_or_b32 v240, v60, s44, v240
  v_and_or_b32 v241, v61, s44, v241
  v_and_or_b32 v242, v62, s44, v242
  v_and_or_b32 v243, v63, s44, v243
  v_mfma_f32_16x16x32_bf16 v[224:227], a[228:231], v[16:19], v[224:227]
  v_cvt_pk_bf16_f32 v30, v240, v241
  v_cvt_pk_bf16_f32 v31, v242, v243
  v_mul_f32_e64 v232, |v92|, s42
  v_mul_f32_e64 v233, |v93|, s42
  v_mul_f32_e64 v234, |v94|, s42
  v_mul_f32_e64 v235, |v95|, s42
  v_mul_f32 v236, s43, v108
  s_waitcnt lgkmcnt(0)
  v_mfma_f32_16x16x32_bf16 v[160:163], a[28:31], a[144:147], v[160:163]
  ds_write_b128 v251, v[28:31] offset:1024
  v_mul_f32 v237, s43, v109
  v_mul_f32 v238, s43, v110
  v_mul_f32 v239, s43, v111
  v_exp_f32 v232, v232
  v_exp_f32 v233, v233
  v_mfma_f32_16x16x32_bf16 v[168:171], a[32:35], a[144:147], v[168:171]
  v_exp_f32 v234, v234
  v_exp_f32 v235, v235
  v_exp_f32 v236, v236
  v_exp_f32 v237, v237
  v_mfma_f32_16x16x32_bf16 v[176:179], a[36:39], a[144:147], v[176:179]
  v_exp_f32 v238, v238
  v_exp_f32 v239, v239
  v_add_f32 v108, 1.0, v232
  v_add_f32 v109, 1.0, v233
  v_add_f32 v110, 1.0, v234
  v_mfma_f32_16x16x32_bf16 v[184:187], a[40:43], a[144:147], v[184:187]
  v_add_f32 v111, 1.0, v235
  v_fma_f32 v236, v108, v236, v108
  v_fma_f32 v237, v109, v237, v109
  v_fma_f32 v238, v110, v238, v110
  v_fma_f32 v239, v111, v239, v111
  v_rcp_f32 v236, v236
  v_mfma_f32_16x16x32_bf16 v[192:195], a[44:47], a[144:147], v[192:195]
  v_rcp_f32 v237, v237
  v_rcp_f32 v238, v238
  v_rcp_f32 v239, v239
  v_fma_f32 v232, -v232, v236, v236
  v_mfma_f32_16x16x32_bf16 v[200:203], a[48:51], a[144:147], v[200:203]
  v_fma_f32 v233, -v233, v237, v237
  v_fma_f32 v234, -v234, v238, v238
  v_fma_f32 v235, -v235, v239, v239
  v_and_or_b32 v232, v92, s44, v232
  v_and_or_b32 v233, v93, s44, v233
  v_and_or_b32 v234, v94, s44, v234
  v_and_or_b32 v235, v95, s44, v235
  v_mfma_f32_16x16x32_bf16 v[208:211], a[52:55], a[144:147], v[208:211]
  v_cvt_pk_bf16_f32 v92, v232, v233
  v_cvt_pk_bf16_f32 v93, v234, v235
  v_mul_f32_e64 v240, |v124|, s42
  v_mul_f32_e64 v241, |v125|, s42
  v_mul_f32_e64 v242, |v126|, s42
  v_mul_f32_e64 v243, |v127|, s42
  v_mul_f32 v244, s43, v140
  v_mfma_f32_16x16x32_bf16 v[216:219], a[56:59], a[144:147], v[216:219]
  v_mul_f32 v245, s43, v141
  v_mul_f32 v246, s43, v142
  v_mul_f32 v247, s43, v143
  v_exp_f32 v240, v240
  v_exp_f32 v241, v241
  v_mfma_f32_16x16x32_bf16 v[224:227], a[60:63], a[144:147], v[224:227]
  v_exp_f32 v242, v242
  v_exp_f32 v243, v243
  v_exp_f32 v244, v244
  v_exp_f32 v245, v245
  v_mfma_f32_16x16x32_bf16 v[160:163], a[64:67], a[152:155], v[160:163]
  v_exp_f32 v246, v246
  v_exp_f32 v247, v247
  v_add_f32 v140, 1.0, v240
  v_add_f32 v141, 1.0, v241
  v_add_f32 v142, 1.0, v242
  v_mfma_f32_16x16x32_bf16 v[168:171], a[68:71], a[152:155], v[168:171]
  v_add_f32 v143, 1.0, v243
  v_fma_f32 v244, v140, v244, v140
  v_fma_f32 v245, v141, v245, v141
  v_fma_f32 v246, v142, v246, v142
  v_fma_f32 v247, v143, v247, v143
  v_rcp_f32 v244, v244
  v_mfma_f32_16x16x32_bf16 v[176:179], a[72:75], a[152:155], v[176:179]
  v_rcp_f32 v245, v245
  v_rcp_f32 v246, v246
  v_rcp_f32 v247, v247
  v_fma_f32 v240, -v240, v244, v244
  v_mfma_f32_16x16x32_bf16 v[184:187], a[76:79], a[152:155], v[184:187]
  v_fma_f32 v241, -v241, v245, v245
  v_fma_f32 v242, -v242, v246, v246
  v_fma_f32 v243, -v243, v247, v247
  v_and_or_b32 v240, v124, s44, v240
  v_and_or_b32 v241, v125, s44, v241
  v_and_or_b32 v242, v126, s44, v242
  v_and_or_b32 v243, v127, s44, v243
  v_mfma_f32_16x16x32_bf16 v[192:195], a[80:83], a[152:155], v[192:195]
  v_cvt_pk_bf16_f32 v94, v240, v241
  v_cvt_pk_bf16_f32 v95, v242, v243
  v_mul_f32_e64 v232, |v20|, s42
  v_mul_f32_e64 v233, |v21|, s42
  v_mul_f32_e64 v234, |v22|, s42
  v_mul_f32_e64 v235, |v23|, s42
  v_mul_f32 v236, s43, v36
  v_mul_f32 v237, s43, v37
  v_mfma_f32_16x16x32_bf16 v[200:203], a[84:87], a[152:155], v[200:203]
  ds_write_b128 v251, v[92:95] offset:3072
  v_mul_f32 v238, s43, v38
  v_mul_f32 v239, s43, v39
  v_exp_f32 v232, v232
  v_exp_f32 v233, v233
  v_exp_f32 v234, v234
  v_mfma_f32_16x16x32_bf16 v[208:211], a[88:91], a[152:155], v[208:211]
  v_exp_f32 v235, v235
  v_exp_f32 v236, v236
  v_exp_f32 v237, v237
  v_mfma_f32_16x16x32_bf16 v[216:219], a[92:95], a[152:155], v[216:219]
  s_waitcnt lgkmcnt(0)
  s_barrier
  v_exp_f32 v238, v238
  v_exp_f32 v239, v239
  v_add_f32 v36, 1.0, v232
  v_add_f32 v37, 1.0, v233
  v_add_f32 v38, 1.0, v234
  v_add_f32 v39, 1.0, v235
  v_mfma_f32_16x16x32_bf16 v[224:227], a[96:99], a[152:155], v[224:227]
  ds_read_b128 a[148:151], v252 offset:1024
  v_fma_f32 v236, v36, v236, v36
  v_fma_f32 v237, v37, v237, v37
  v_fma_f32 v238, v38, v238, v38
  v_fma_f32 v239, v39, v239, v39
  v_rcp_f32 v236, v236
  v_rcp_f32 v237, v237
  v_mfma_f32_16x16x32_bf16 v[160:163], a[160:163], v[80:83], v[160:163]
  ds_read_b128 a[156:159], v252 offset:3072
  v_rcp_f32 v238, v238
  v_rcp_f32 v239, v239
  v_fma_f32 v232, -v232, v236, v236
  v_mfma_f32_16x16x32_bf16 v[168:171], a[164:167], v[80:83], v[168:171]
  v_fma_f32 v233, -v233, v237, v237
  v_fma_f32 v234, -v234, v238, v238
  v_fma_f32 v235, -v235, v239, v239
  v_and_or_b32 v232, v20, s44, v232
  v_mfma_f32_16x16x32_bf16 v[176:179], a[168:171], v[80:83], v[176:179]
  v_and_or_b32 v233, v21, s44, v233
  v_and_or_b32 v234, v22, s44, v234
  v_mfma_f32_16x16x32_bf16 v[184:187], a[172:175], v[80:83], v[184:187]
  v_and_or_b32 v235, v23, s44, v235
  v_cvt_pk_bf16_f32 v20, v232, v233
  v_cvt_pk_bf16_f32 v21, v234, v235
  v_cvt_pk_bf16_f32 v32, v160, v161
  v_mfma_f32_16x16x32_bf16 v[192:195], a[176:179], v[80:83], v[192:195]
  v_cvt_pk_bf16_f32 v34, v168, v169
  v_cvt_pk_bf16_f32 v33, v162, v163
  v_mfma_f32_16x16x32_bf16 v[200:203], a[180:183], v[80:83], v[200:203]
  v_cvt_pk_bf16_f32 v35, v170, v171
  v_cvt_pk_bf16_f32 v64, v176, v177
  v_cvt_pk_bf16_f32 v66, v184, v185
  v_cvt_pk_bf16_f32 v65, v178, v179
  v_mfma_f32_16x16x32_bf16 v[208:211], a[184:187], v[80:83], v[208:211]
  v_cvt_pk_bf16_f32 v67, v186, v187
  v_permlane16_swap_b32 v32, v34
  v_permlane16_swap_b32 v33, v35
  v_mfma_f32_16x16x32_bf16 v[216:219], a[188:191], v[80:83], v[216:219]
  v_cvt_pk_bf16_f32 v96, v192, v193
  v_cvt_pk_bf16_f32 v98, v200, v201
  v_cvt_pk_bf16_f32 v97, v194, v195
  v_cvt_pk_bf16_f32 v99, v202, v203
  v_mfma_f32_16x16x32_bf16 v[224:227], a[192:195], v[80:83], v[224:227]
  buffer_store_dwordx4 v[32:35], v14, s[28:31], 0 offen
  v_permlane16_swap_b32 v64, v66
  v_permlane16_swap_b32 v65, v67
  v_cvt_pk_bf16_f32 v128, v208, v209
  v_cvt_pk_bf16_f32 v130, v216, v217
  v_cvt_pk_bf16_f32 v129, v210, v211
  s_waitcnt lgkmcnt(0)
  v_mfma_f32_16x16x32_bf16 v[164:167], a[28:31], a[148:151], v[164:167]
  v_cvt_pk_bf16_f32 v131, v218, v219
  v_mfma_f32_16x16x32_bf16 v[172:175], a[32:35], a[148:151], v[172:175]
  buffer_store_dwordx4 v[64:67], v14, s[28:31], 0 offen offset:64
  v_permlane16_swap_b32 v96, v98
  v_permlane16_swap_b32 v97, v99
  v_cvt_pk_bf16_f32 v40, v224, v225
  v_cvt_pk_bf16_f32 v41, v226, v227
  v_mfma_f32_16x16x32_bf16 v[180:183], a[36:39], a[148:151], v[180:183]
  buffer_store_dwordx4 v[96:99], v14, s[28:31], 0 offen offset:128
  v_permlane16_swap_b32 v128, v130
  v_permlane16_swap_b32 v129, v131
  v_mfma_f32_16x16x32_bf16 v[188:191], a[40:43], a[148:151], v[188:191]
  buffer_store_dwordx2 v[40:41], v15, s[28:31], 0 offen
  v_mfma_f32_16x16x32_bf16 v[196:199], a[44:47], a[148:151], v[196:199]
  buffer_store_dwordx4 v[128:131], v14, s[28:31], 0 offen offset:192
  v_mul_f32_e64 v240, |v52|, s42
  v_mul_f32_e64 v241, |v53|, s42
  v_mul_f32_e64 v242, |v54|, s42
  v_mul_f32_e64 v243, |v55|, s42
  v_mul_f32 v244, s43, v68
  v_mul_f32 v245, s43, v69
  v_mfma_f32_16x16x32_bf16 v[204:207], a[48:51], a[148:151], v[204:207]
  v_mul_f32 v246, s43, v70
  v_mul_f32 v247, s43, v71
  v_exp_f32 v240, v240
  v_exp_f32 v241, v241
  v_mfma_f32_16x16x32_bf16 v[212:215], a[52:55], a[148:151], v[212:215]
  v_exp_f32 v242, v242
  v_exp_f32 v243, v243
  v_exp_f32 v244, v244
  v_mfma_f32_16x16x32_bf16 v[220:223], a[56:59], a[148:151], v[220:223]
  v_exp_f32 v245, v245
  v_exp_f32 v246, v246
  v_exp_f32 v247, v247
  v_mfma_f32_16x16x32_bf16 v[228:231], a[60:63], a[148:151], v[228:231]
  v_add_f32 v68, 1.0, v240
  v_add_f32 v69, 1.0, v241
  v_add_f32 v70, 1.0, v242
  v_add_f32 v71, 1.0, v243
  v_fma_f32 v244, v68, v244, v68
  v_fma_f32 v245, v69, v245, v69
  v_mfma_f32_16x16x32_bf16 v[164:167], a[64:67], a[156:159], v[164:167]
  v_fma_f32 v246, v70, v246, v70
  v_fma_f32 v247, v71, v247, v71
  v_rcp_f32 v244, v244
  v_rcp_f32 v245, v245
  v_mfma_f32_16x16x32_bf16 v[172:175], a[68:71], a[156:159], v[172:175]
  v_rcp_f32 v246, v246
  v_rcp_f32 v247, v247
  v_fma_f32 v240, -v240, v244, v244
  v_fma_f32 v241, -v241, v245, v245
  v_mfma_f32_16x16x32_bf16 v[180:183], a[72:75], a[156:159], v[180:183]
  v_fma_f32 v242, -v242, v246, v246
  v_fma_f32 v243, -v243, v247, v247
  v_and_or_b32 v240, v52, s44, v240
  v_and_or_b32 v241, v53, s44, v241
  v_and_or_b32 v242, v54, s44, v242
  v_and_or_b32 v243, v55, s44, v243
  v_mfma_f32_16x16x32_bf16 v[188:191], a[76:79], a[156:159], v[188:191]
  v_cvt_pk_bf16_f32 v22, v240, v241
  v_cvt_pk_bf16_f32 v23, v242, v243
  v_mul_f32_e64 v232, |v84|, s42
  v_mul_f32_e64 v233, |v85|, s42
  v_mul_f32_e64 v234, |v86|, s42
  v_mul_f32_e64 v235, |v87|, s42
  v_mfma_f32_16x16x32_bf16 v[196:199], a[80:83], a[156:159], v[196:199]
  v_mul_f32 v236, s43, v100
  v_mul_f32 v237, s43, v101
  v_mul_f32 v238, s43, v102
  v_mul_f32 v239, s43, v103
  v_exp_f32 v232, v232
  v_mfma_f32_16x16x32_bf16 v[204:207], a[84:87], a[156:159], v[204:207]
  v_exp_f32 v233, v233
  v_exp_f32 v234, v234
  v_exp_f32 v235, v235
  v_mfma_f32_16x16x32_bf16 v[212:215], a[88:91], a[156:159], v[212:215]
  v_exp_f32 v236, v236
  v_exp_f32 v237, v237
  v_exp_f32 v238, v238
  v_mfma_f32_16x16x32_bf16 v[220:223], a[92:95], a[156:159], v[220:223]
  v_exp_f32 v239, v239
  v_add_f32 v100, 1.0, v232
  v_add_f32 v101, 1.0, v233
  v_add_f32 v102, 1.0, v234
  v_add_f32 v103, 1.0, v235
  v_mfma_f32_16x16x32_bf16 v[228:231], a[96:99], a[156:159], v[228:231]
  v_fma_f32 v236, v100, v236, v100
  v_fma_f32 v237, v101, v237, v101
  v_fma_f32 v238, v102, v238, v102
  v_fma_f32 v239, v103, v239, v103
  v_rcp_f32 v236, v236
  v_mfma_f32_16x16x32_bf16 v[164:167], a[196:199], v[20:23], v[164:167]
  v_rcp_f32 v237, v237
  v_rcp_f32 v238, v238
  v_rcp_f32 v239, v239
  v_mfma_f32_16x16x32_bf16 v[172:175], a[200:203], v[20:23], v[172:175]
  v_fma_f32 v232, -v232, v236, v236
  v_fma_f32 v233, -v233, v237, v237
  v_fma_f32 v234, -v234, v238, v238
  v_fma_f32 v235, -v235, v239, v239
  v_and_or_b32 v232, v84, s44, v232
  v_and_or_b32 v233, v85, s44, v233
  v_mfma_f32_16x16x32_bf16 v[180:183], a[204:207], v[20:23], v[180:183]
  v_and_or_b32 v234, v86, s44, v234
  v_and_or_b32 v235, v87, s44, v235
  v_cvt_pk_bf16_f32 v84, v232, v233
  v_cvt_pk_bf16_f32 v85, v234, v235
  v_mul_f32_e64 v240, |v116|, s42
  v_mul_f32_e64 v241, |v117|, s42
  v_mfma_f32_16x16x32_bf16 v[188:191], a[208:211], v[20:23], v[188:191]
  v_mul_f32_e64 v242, |v118|, s42
  v_mul_f32_e64 v243, |v119|, s42
  v_mul_f32 v244, s43, v132
  v_mul_f32 v245, s43, v133
  v_mul_f32 v246, s43, v134
  v_mul_f32 v247, s43, v135
  v_mfma_f32_16x16x32_bf16 v[196:199], a[212:215], v[20:23], v[196:199]
  v_exp_f32 v240, v240
  v_exp_f32 v241, v241
  v_exp_f32 v242, v242
  v_mfma_f32_16x16x32_bf16 v[204:207], a[216:219], v[20:23], v[204:207]
  v_exp_f32 v243, v243
  v_exp_f32 v244, v244
  v_exp_f32 v245, v245
  v_mfma_f32_16x16x32_bf16 v[212:215], a[220:223], v[20:23], v[212:215]
  v_exp_f32 v246, v246
  v_exp_f32 v247, v247
  v_add_f32 v132, 1.0, v240
  v_add_f32 v133, 1.0, v241
  v_mfma_f32_16x16x32_bf16 v[220:223], a[224:227], v[20:23], v[220:223]
  v_add_f32 v134, 1.0, v242
  v_add_f32 v135, 1.0, v243
  v_fma_f32 v244, v132, v244, v132
  v_fma_f32 v245, v133, v245, v133
  v_fma_f32 v246, v134, v246, v134
  v_fma_f32 v247, v135, v247, v135
  v_mfma_f32_16x16x32_bf16 v[228:231], a[228:231], v[20:23], v[228:231]
  v_rcp_f32 v244, v244
  v_rcp_f32 v245, v245
  v_rcp_f32 v246, v246
  v_rcp_f32 v247, v247
  v_fma_f32 v240, -v240, v244, v244
  v_fma_f32 v241, -v241, v245, v245
  v_fma_f32 v242, -v242, v246, v246
  v_fma_f32 v243, -v243, v247, v247
  v_and_or_b32 v240, v116, s44, v240
  v_and_or_b32 v241, v117, s44, v241
  v_and_or_b32 v242, v118, s44, v242
  v_and_or_b32 v243, v119, s44, v243
  v_cvt_pk_bf16_f32 v86, v240, v241
  v_cvt_pk_bf16_f32 v87, v242, v243
  s_nop 1
  v_mfma_f32_16x16x32_bf16 v[164:167], a[160:163], v[84:87], v[164:167]
  v_mfma_f32_16x16x32_bf16 v[172:175], a[164:167], v[84:87], v[172:175]
  v_mfma_f32_16x16x32_bf16 v[180:183], a[168:171], v[84:87], v[180:183]
  s_nop 5
  v_cvt_pk_bf16_f32 v164, v164, v165
  v_cvt_pk_bf16_f32 v165, v166, v167
  v_mfma_f32_16x16x32_bf16 v[188:191], a[172:175], v[84:87], v[188:191]
  v_cvt_pk_bf16_f32 v172, v172, v173
  v_cvt_pk_bf16_f32 v173, v174, v175
  v_mfma_f32_16x16x32_bf16 v[196:199], a[176:179], v[84:87], v[196:199]
  v_cvt_pk_bf16_f32 v180, v180, v181
  v_cvt_pk_bf16_f32 v181, v182, v183
  v_cndmask_b32 v36, v164, v172, vcc
  v_cndmask_b32 v37, v165, v173, vcc
  v_cndmask_b32 v38, v172, v180, vcc
  v_cndmask_b32 v39, v173, v181, vcc
  v_mfma_f32_16x16x32_bf16 v[204:207], a[180:183], v[84:87], v[204:207]
  v_cvt_pk_bf16_f32 v188, v188, v189
  v_cvt_pk_bf16_f32 v189, v190, v191
  v_cvt_pk_bf16_f32 v196, v196, v197
  v_cvt_pk_bf16_f32 v197, v198, v199
  v_mfma_f32_16x16x32_bf16 v[212:215], a[184:187], v[84:87], v[212:215]
  v_permlane16_swap_b32 v36, v38
  v_permlane16_swap_b32 v37, v39
  v_cndmask_b32 v68, v180, v188, vcc
  v_cndmask_b32 v69, v181, v189, vcc
  v_cndmask_b32 v70, v188, v196, vcc
  v_cndmask_b32 v71, v189, v197, vcc
  v_mfma_f32_16x16x32_bf16 v[220:223], a[188:191], v[84:87], v[220:223]
  buffer_store_dwordx4 v[36:39], v253, s[28:31], s48 offen
  v_cvt_pk_bf16_f32 v204, v204, v205
  v_cvt_pk_bf16_f32 v205, v206, v207
  v_cvt_pk_bf16_f32 v212, v212, v213
  v_cvt_pk_bf16_f32 v213, v214, v215
  v_mfma_f32_16x16x32_bf16 v[228:231], a[192:195], v[84:87], v[228:231]
  v_permlane16_swap_b32 v68, v70
  v_permlane16_swap_b32 v69, v71
  v_cndmask_b32 v100, v196, v204, vcc
  v_cndmask_b32 v101, v197, v205, vcc
  v_cndmask_b32 v102, v204, v212, vcc
  v_cndmask_b32 v103, v205, v213, vcc
  buffer_store_dwordx4 v[68:71], v253, s[28:31], s48 offen offset:64
  v_cvt_pk_bf16_f32 v220, v220, v221
  v_cvt_pk_bf16_f32 v221, v222, v223
  v_cvt_pk_bf16_f32 v228, v228, v229
  v_cvt_pk_bf16_f32 v229, v230, v231
  v_permlane16_swap_b32 v100, v102
  v_permlane16_swap_b32 v101, v103
  v_cndmask_b32 v132, v212, v220, vcc
  v_cndmask_b32 v133, v213, v221, vcc
  v_cndmask_b32 v134, v220, v228, vcc
  v_cndmask_b32 v135, v221, v229, vcc
  buffer_store_dwordx4 v[100:103], v253, s[28:31], s48 offen offset:128
  v_cndmask_b32 v44, v228, v164, vcc
  v_cndmask_b32 v45, v229, v165, vcc
  v_permlane16_swap_b32 v132, v134
  v_permlane16_swap_b32 v133, v135
  buffer_store_dwordx2 v[44:45], v254, s[28:31], s48 offen
  buffer_store_dwordx4 v[132:135], v253, s[28:31], s48 offen offset:192
CODE_END_wavenet_fused_kernel:
)ASM" : "+{v0}"(tid), "+{s14}"(bid) : "{s[0:1]}"(__builtin_amdgcn_kernarg_segment_ptr()), "s"((unsigned)(size_t)smem)
  : "memory", "vcc", "scc", "s4", "s5", "s6", "s7", "s8", "s9", "s10", "s11", "v1", "v2", "v3", "v4", "v5", "v6", "v7", "v8", "v9", "v10", "v11", "v12", "v13", "v14", "v15", "v16", "v17", "v18", "v19", "v20", "v21", "v22", "v23", "v24", "v25", "v26", "v27", "v28", "v29", "v30", "v31", "v32", "v33", "v34", "v35", "v36", "v37", "v38", "v39", "v40", "v41", "v42", "v43", "v44", "v45", "v46", "v47", "v48", "v49", "v50", "v51", "v52", "v53", "v54", "v55", "v56", "v57", "v58", "v59", "v60", "v61", "v62", "v63", "v64", "v65", "v66", "v67", "v68", "v69", "v70", "v71", "v72", "v73", "v74", "v75", "v76", "v77", "v78", "v79", "v80", "v81", "v82", "v83", "v84", "v85", "v86", "v87", "v88", "v89", "v90", "v91", "v92", "v93", "v94", "v95", "v96", "v97", "v98", "v99", "v100", "v101", "v102", "v103", "v104", "v105", "v106", "v107", "v108", "v109", "v110", "v111", "v112", "v113", "v114", "v115", "v116", "v117", "v118", "v119", "v120", "v121", "v122", "v123", "v124", "v125", "v126", "v127", "v128", "v129", "v130", "v131", "v132", "v133", "v134", "v135", "v136", "v137", "v138", "v139", "v140", "v141", "v142", "v143", "v144", "v145", "v146", "v147", "v148", "v149", "v150", "v151", "v152", "v153", "v154", "v155", "v156", "v157", "v158", "v159", "v160", "v161", "v162", "v163", "v164", "v165", "v166", "v167", "v168", "v169", "v170", "v171", "v172", "v173", "v174", "v175", "v176", "v177", "v178", "v179", "v180", "v181", "v182", "v183", "v184", "v185", "v186", "v187", "v188", "v189", "v190", "v191", "v192", "v193", "v194", "v195", "v196", "v197", "v198", "v199", "v200", "v201", "v202", "v203", "v204", "v205", "v206", "v207", "v208", "v209", "v210", "v211", "v212", "v213", "v214", "v215", "v216", "v217", "v218", "v219", "v220", "v221", "v222", "v223", "v224", "v225", "v226", "v227", "v228", "v229", "v230", "v231", "v232", "v233", "v234", "v235", "v236", "v237", "v238", "v239", "v240", "v241", "v242", "v243", "v244", "v245", "v246", "v247", "v248", "v249", "v250", "v251", "v252", "v253", "v254", "v255", "a0", "a1", "a2", "a3", "a4", "a5", "a6", "a7", "a8", "a9", "a10", "a11", "a12", "a13", "a14", "a15", "a16", "a17", "a18", "a19", "a20", "a21", "a22", "a23", "a24", "a25", "a26", "a27", "a28", "a29", "a30", "a31", "a32", "a33", "a34", "a35", "a36", "a37", "a38", "a39", "a40", "a41", "a42", "a43", "a44", "a45", "a46", "a47", "a48", "a49", "a50", "a51", "a52", "a53", "a54", "a55", "a56", "a57", "a58", "a59", "a60", "a61", "a62", "a63", "a64", "a65", "a66", "a67", "a68", "a69", "a70", "a71", "a72", "a73", "a74", "a75", "a76", "a77", "a78", "a79", "a80", "a81", "a82", "a83", "a84", "a85", "a86", "a87", "a88", "a89", "a90", "a91", "a92", "a93", "a94", "a95", "a96", "a97", "a98", "a99", "a100", "a101", "a102", "a103", "a104", "a105", "a106", "a107", "a108", "a109", "a110", "a111", "a112", "a113", "a114", "a115", "a116", "a117", "a118", "a119", "a120", "a121", "a122", "a123", "a124", "a125", "a126", "a127", "a128", "a129", "a130", "a131", "a132", "a133", "a134", "a135", "a136", "a137", "a138", "a139", "a140", "a141", "a142", "a143", "a144", "a145", "a146", "a147", "a148", "a149", "a150", "a151", "a152", "a153", "a154", "a155", "a156", "a157", "a158", "a159", "a160", "a161", "a162", "a163", "a164", "a165", "a166", "a167", "a168", "a169", "a170", "a171", "a172", "a173", "a174", "a175", "a176", "a177", "a178", "a179", "a180", "a181", "a182", "a183", "a184", "a185", "a186", "a187", "a188", "a189", "a190", "a191", "a192", "a193", "a194", "a195", "a196", "a197", "a198", "a199", "a200", "a201", "a202", "a203", "a204", "a205", "a206", "a207", "a208", "a209", "a210", "a211", "a212", "a213", "a214", "a215", "a216", "a217", "a218", "a219", "a220", "a221", "a222", "a223", "a224", "a225", "a226", "a227", "a228", "a229", "a230", "a231", "a232", "a233", "a234", "a235", "a236", "a237", "a238", "a239", "a240", "a241", "a242", "a243", "a244", "a245", "a246", "a247", "a248", "a249", "a250", "a251", "a252", "a253", "a254", "a255", "s15", "s16", "s17", "s18", "s19", "s20", "s21", "s22", "s23", "s24", "s25", "s26", "s27", "s28", "s29", "s30", "s31", "s34", "s35", "s36", "s37", "s38", "s39", "s40", "s41", "s42", "s43", "s44", "s45", "s46", "s47", "s48", "s49", "s50", "s51", "s52", "s53", "s54", "s55", "s56", "s57", "s58", "s59", "s60", "s61", "s62", "s63", "s64", "s65", "s66", "s67", "s68", "s69", "s70", "s71", "s72", "s73", "s74", "s75", "s76", "s77", "s78", "s79", "s80", "s81", "s82", "s83", "s84", "s85", "s86", "s87", "s88", "s89", "s90", "s91", "s92", "s93", "s94", "s95", "s12", "s13");
}

extern "C" void kernel_launch(void* const* d_in, const int* in_sizes, int n_in,
                              void* d_out, int out_size, void* d_ws, size_t ws_size,
                              hipStream_t stream) {
    wavenet_fused_kernel<<<250, 256, 0, stream>>>(d_in[0], d_in[1], d_in[2], d_out);
}
```
